```python
import torch

def _softmax(x, axis=-1):
    return torch.softmax(x, dim=axis)

def _gelu(x):
    return torch.nn.functional.gelu(x, approximate='tanh')

def _silu(x):
    return torch.nn.functional.silu(x)

def _sigmoid(x):
    return torch.sigmoid(x)

def _layernorm(x, axis=-1, eps=1e-05):
    m = x.mean(dim=axis, keepdim=True)
    v = x.var(dim=axis, keepdim=True, unbiased=False)
    return (x - m) / torch.sqrt(v + eps)

def _scatter_add(tgt, idx, src, axis=0):
    return tgt.clone().index_add_(axis, idx, src)

def _segment_sum(src, seg, n):
    out = torch.zeros((n,) + src.shape[1:], dtype=src.dtype)
    return out.index_add_(0, seg, src)

def _pad(x, pw):
    flat = tuple((v for pair in reversed(pw) for v in pair))
    return torch.nn.functional.pad(x, flat)
kernel_name = 'gated_mix__grouped_kv_mqa__mixer_gmlp__prompt'

def setup_inputs():

    _w = lambda a: (lambda t: t.to(torch.bfloat16) if t.is_floating_point() else t)(torch.as_tensor(a))

    def _A_setup():
        B, S, D, H = (1, 2048, 576, 32)
        Dh = D // H
        return {'x': _w((lambda *s: torch.randn(*s, dtype=torch.bfloat16) * 0.05)(B, S, D)), 'wq': _w((lambda *s: torch.randn(*s, dtype=torch.bfloat16) * 3.2)(D, H * Dh)), 'wk': _w((lambda *s: torch.randn(*s, dtype=torch.bfloat16) * 3.2)(D, Dh)), 'wv': _w((lambda *s: torch.randn(*s, dtype=torch.bfloat16) * 0.4)(D, Dh)), 'wo': _w((lambda *s: torch.randn(*s, dtype=torch.bfloat16) * 0.4)(H * Dh, D))}
    _A_in = _A_setup()

    def _B_setup():
        B, S, D, F = (1, 2048, 576, 4608)
        Fh = F // 2
        return {'x': _w((lambda *s: torch.randn(*s, dtype=torch.bfloat16) * 0.05)(B, S, D)), 'w_in': _w((lambda *s: torch.randn(*s, dtype=torch.bfloat16) * 1.6)(D, 2 * Fh)), 'w_sp': _w(0.01 * (lambda *s: torch.randn(*s, dtype=torch.bfloat16) * 1.6)(S, S)), 'b_sp': _w(1.0 + 0.01 * (lambda *s: torch.randn(*s, dtype=torch.bfloat16) * 0.1)(S)), 'w_out': _w((lambda *s: torch.randn(*s, dtype=torch.bfloat16) * 0.1)(Fh, D))}
    _B_in = _B_setup()
    return {'A_x': _A_in['x'], 'A_wq': _A_in['wq'], 'A_wk': _A_in['wk'], 'A_wv': _A_in['wv'], 'A_wo': _A_in['wo'], 'B_x': _B_in['x'], 'B_w_in': _B_in['w_in'], 'B_w_sp': _B_in['w_sp'], 'B_b_sp': _B_in['b_sp'], 'B_w_out': _B_in['w_out'], 'gate_w': _w((lambda *s: torch.randn(*s, dtype=torch.bfloat16) * 0.1)(576) * 16.0)}

def reference(A_x, A_wq, A_wk, A_wv, A_wo, B_x, B_w_in, B_w_sp, B_b_sp, B_w_out, gate_w):

    def _A_ref(x, wq, wk, wv, wo):
        B, S, D = x.shape
        H = 32
        Dh = D // H
        q = (lambda x, *d: x.permute(*d))((x @ wq).reshape(B, S, H, Dh), 0, 2, 1, 3)
        k = x @ wk
        v = x @ wv
        scores = torch.einsum('bhqd,bkd->bhqk', q, k) * Dh ** (-0.5)
        p = _softmax(scores, axis=-1)
        o = torch.einsum('bhqk,bkd->bhqd', p, v)
        return (lambda x, *d: x.permute(*d))(o, 0, 2, 1, 3).reshape(B, S, H * Dh) @ wo

    def _B_ref(x, w_in, w_sp, b_sp, w_out):
        B, S, D = x.shape
        F = w_in.shape[-1]
        Fh = F // 2
        z = _gelu(torch.einsum('bsd,df->bsf', x, w_in))
        z1 = z[..., :Fh].clone()
        z2 = _layernorm(z[..., Fh:], axis=-1)
        g = torch.einsum('ts,bsf->btf', w_sp, z2) + b_sp[None, :, None]
        return torch.einsum('bsf,fd->bsd', z1 * g, w_out)
    x = A_x
    g = _sigmoid((x * gate_w).sum(-1, keepdims=True))
    a = _A_ref(x, A_wq, A_wk, A_wv, A_wo)
    b = _B_ref(x, B_w_in, B_w_sp, B_b_sp, B_w_out)
    return g * a + (1 - g) * b
```

```cpp
#include <hip/hip_runtime.h>
#include <stdint.h>

typedef __attribute__((ext_vector_type(8))) __bf16 bfx8;
typedef __attribute__((ext_vector_type(2))) __bf16 bfx2;
typedef __attribute__((ext_vector_type(4))) short s16x4;
typedef __attribute__((ext_vector_type(4))) float f32x4;
typedef __attribute__((ext_vector_type(16))) float f32x16;
typedef __attribute__((ext_vector_type(4))) unsigned u32x4;
typedef __attribute__((ext_vector_type(2))) unsigned u32x2;
typedef u32x4 __attribute__((aligned(4))) u32x4_a4;
typedef unsigned short u16;
#define LDSP __attribute__((address_space(3)))
#define GLBP __attribute__((address_space(1)))

constexpr int S_ = 2048, D_ = 576, FH = 2304;
constexpr size_t WS_Q   = 0;
constexpr size_t WS_KV  = WS_Q + (size_t)S_ * D_ * 2;
constexpr size_t WS_G   = WS_KV + 64 * 3584;
constexpr size_t WS_Z2  = WS_G + 2048 * 4;
constexpr size_t WS_W1  = WS_Z2 + (size_t)144 * 64 * 1024;
constexpr size_t WS_WO  = WS_W1 + (size_t)144 * 18 * 1024;
constexpr size_t WS_AH  = WS_WO + (size_t)36 * 90 * 1024;

__device__ __forceinline__ unsigned pk2(float a, float b) { bfx2 r; r[0] = (__bf16)a; r[1] = (__bf16)b; return __builtin_bit_cast(unsigned, r); }
__device__ __forceinline__ float bflo(unsigned u) { return __uint_as_float(u << 16); }
__device__ __forceinline__ float bfhi(unsigned u) { return __uint_as_float(u & 0xffff0000u); }
__device__ __forceinline__ float gelu_t(float x) {
  const float c0 = -2.302208198f;
  const float c1 = c0 * 0.044715f;
  float e = __builtin_amdgcn_exp2f(x * __builtin_fmaf(x * x, c1, c0));
  return x * __builtin_amdgcn_rcpf(1.0f + e);
}
__device__ __forceinline__ void glds16(const void* g, char* l) {
  __builtin_amdgcn_global_load_lds((const GLBP void*)g, (LDSP void*)l, 16, 0, 0);
}
#define MFMA16(a, b, c) __builtin_amdgcn_mfma_f32_16x16x32_bf16(a, b, c, 0, 0, 0)
#define MFMA32(a, b, c) __builtin_amdgcn_mfma_f32_32x32x16_bf16(a, b, c, 0, 0, 0)
#define MFMA32H(a, b, c) __builtin_amdgcn_mfma_f32_32x32x8bf16_1k(a, b, c, 0, 0, 0)
#define WAITV(n) asm volatile("s_waitcnt vmcnt(" #n ")" ::: "memory")
#define WAITL0() asm volatile("s_waitcnt lgkmcnt(0)" ::: "memory")
#define BAR() __builtin_amdgcn_s_barrier()
#define SCHED0() __builtin_amdgcn_sched_barrier(0)

constexpr int K1_ST = 40960, K1_NS = 3;

template <bool ISQ>
__device__ __forceinline__ void k1_consumer(char* sm, int w, int lane, int rt, int ct, char* ws) {
  const int wm = w & 1, wn = w >> 1, i = lane & 15, g = lane >> 4;
  f32x4 acc[4][6];
#pragma unroll
  for (int m = 0; m < 4; m++)
#pragma unroll
    for (int n = 0; n < 6; n++) acc[m][n] = f32x4{0.f, 0.f, 0.f, 0.f};
  const int arow = wm * 64 + 8 * (i >> 2) + (i & 3);
  const int aoff0 = arow * 128 + ((g ^ (i & 7)) * 16), aoff1 = aoff0 ^ 64;
  int boff[6];
  {
    const int q = i >> 2, p = i & 3, h = ((q >> 1) & 1) | ((g & 1) << 1);
#pragma unroll
    for (int n = 0; n < 6; n++) {
      int nt = wn * 6 + n;
      boff[n] = 16384 + (nt >> 2) * 8192 + g * 1024 + q * 128 + (((nt & 3) ^ h) * 32) + p * 8;
    }
  }
  bfx8 Af[2][4], Bf[2][6];
  auto rd = [&](int buf, char* st, int ks) {
#pragma unroll
    for (int n = 0; n < 6; n++) {
      s16x4 lo = __builtin_amdgcn_ds_read_tr16_b64_v4i16((LDSP s16x4*)(st + boff[n] + ks * 4096));
      s16x4 hi = __builtin_amdgcn_ds_read_tr16_b64_v4i16((LDSP s16x4*)(st + boff[n] + ks * 4096 + 512));
      Bf[buf][n] = __builtin_bit_cast(bfx8, __builtin_shufflevector(lo, hi, 0, 1, 2, 3, 4, 5, 6, 7));
    }
#pragma unroll
    for (int m = 0; m < 4; m++)
      Af[buf][m] = *(const bfx8*)(st + (ks ? aoff1 : aoff0) + (4 * (m & 1) + 32 * (m >> 1)) * 128);
  };
  auto mm = [&](int buf) {
#pragma unroll
    for (int m = 0; m < 4; m++)
#pragma unroll
      for (int n = 0; n < 6; n++)
        acc[m][n] = ISQ ? MFMA16(Bf[buf][n], Af[buf][m], acc[m][n]) : MFMA16(Af[buf][m], Bf[buf][n], acc[m][n]);
  };
  auto ilv = [&]() {
#pragma unroll
    for (int x = 0; x < 8; x++) {
      __builtin_amdgcn_sched_group_barrier(0x8, 2, 0);
      __builtin_amdgcn_sched_group_barrier(0x100, 2, 0);
    }
    __builtin_amdgcn_sched_group_barrier(0x8, 8, 0);
  };
  BAR(); SCHED0();
  rd(0, sm, 0);
  SCHED0();
#pragma unroll
  for (int kt = 0; kt < 9; kt++) {
    char* st = sm + (kt % K1_NS) * K1_ST;
    rd(1, st, 1); mm(0); ilv();
    SCHED0(); WAITL0(); BAR(); SCHED0();
    if (kt < 8) rd(0, sm + ((kt + 1) % K1_NS) * K1_ST, 0);
    mm(1);
    if (kt < 8) ilv();
    SCHED0();
  }
  if (!ISQ) {
    char* z2 = ws + WS_Z2;
#pragma unroll
    for (int n = 0; n < 6; n++)
#pragma unroll
      for (int mp = 0; mp < 2; mp++) {
        f32x4 a = acc[2 * mp][n], b = acc[2 * mp + 1][n];
        u32x4 o = {pk2(gelu_t(a[0]), gelu_t(a[1])), pk2(gelu_t(a[2]), gelu_t(a[3])),
                   pk2(gelu_t(b[0]), gelu_t(b[1])), pk2(gelu_t(b[2]), gelu_t(b[3]))};
        int ft = ct * 12 + wn * 6 + n, kt32 = rt * 4 + wm * 2 + mp;
        *(u32x4*)(z2 + ((size_t)(ft * 64 + kt32) * 64 + lane) * 16) = o;
      }
  } else {
    const float c = 0.34004649f;
    char* q = ws + WS_Q;
#pragma unroll
    for (int m = 0; m < 4; m++) {
      int t = rt * 128 + arow + 4 * (m & 1) + 32 * (m >> 1);
#pragma unroll
      for (int n = 0; n < 6; n++) {
        f32x4 a = acc[m][n];
        u32x2 o = {pk2(a[0] * c, a[1] * c), pk2(a[2] * c, a[3] * c)};
        *(u32x2*)(q + ((size_t)t * 576 + (ct - 12) * 192 + wn * 96 + n * 16 + g * 4) * 2) = o;
      }
    }
  }
}

__global__ __launch_bounds__(512, 2) void k1_proj(const u16* __restrict__ x, const u16* __restrict__ wq,
                                                  const u16* __restrict__ wk, const u16* __restrict__ wv,
                                                  const u16* __restrict__ w_in, const u16* __restrict__ gw,
                                                  char* __restrict__ ws) {
  __shared__ __attribute__((aligned(1024))) char sm[K1_ST * K1_NS];
  const int tid = threadIdx.x, lane = tid & 63;
  const int w = __builtin_amdgcn_readfirstlane(tid >> 6);
  const int b = blockIdx.x, xcd = b & 7, ii = b >> 3;
  const int rt = (xcd & 1) * 8 + (ii & 7), ct = (xcd >> 1) * 4 + (ii >> 3);
  if (ct == 15) {
    const int i = lane & 15, g = lane >> 4;
    bfx8 Af[18];
    const u16* xr = x + (size_t)(rt * 128 + w * 16 + i) * 576 + g * 8;
#pragma unroll
    for (int k = 0; k < 18; k++) Af[k] = *(const bfx8*)(xr + k * 32);
    for (int fr = w; fr < 54; fr += 8) {
      int nt = fr / 18, kt = fr % 18, n = nt * 16 + i, k0 = kt * 32 + g * 8;
      const u16* src = n < 18 ? wk + n : (n < 36 ? wv + (n - 18) : gw);
      int str = n < 36 ? 18 : 1;
      unsigned v[8];
#pragma unroll
      for (int j = 0; j < 8; j++) v[j] = src[(k0 + j) * str];
      u32x4 o = {v[0] | (v[1] << 16), v[2] | (v[3] << 16), v[4] | (v[5] << 16), v[6] | (v[7] << 16)};
      if (n > 36) o = u32x4{0, 0, 0, 0};
      *(u32x4*)(sm + fr * 1024 + lane * 16) = o;
    }
    __syncthreads();
    f32x4 acc[3] = {{0.f, 0.f, 0.f, 0.f}, {0.f, 0.f, 0.f, 0.f}, {0.f, 0.f, 0.f, 0.f}};
#pragma unroll
    for (int k = 0; k < 18; k++)
#pragma unroll
      for (int n = 0; n < 3; n++) acc[n] = MFMA16(Af[k], *(const bfx8*)(sm + (n * 18 + k) * 1024 + lane * 16), acc[n]);
    float* res = (float*)(sm + 56 * 1024);
#pragma unroll
    for (int n = 0; n < 3; n++)
#pragma unroll
      for (int r = 0; r < 4; r++) res[(w * 16 + g * 4 + r) * 49 + n * 16 + i] = acc[n][r];
    __syncthreads();
    char* kv = ws + WS_KV + (size_t)rt * 4 * 3584;
    if (tid < 128) {
      float s = res[tid * 49 + 36];
      ((float*)(ws + WS_G))[rt * 128 + tid] = __builtin_amdgcn_rcpf(1.0f + __builtin_amdgcn_exp2f(-1.44269504f * s));
    }
    {
      const int tile = w >> 1, r = lane & 31, h = lane >> 5;
      const float* rb = res + tile * 32 * 49;
      char* dst = kv + tile * 3584;
      if ((w & 1) == 0) {
        const float* p = rb + r * 49 + 8 * h;
        u32x4 o = {pk2(p[0], p[1]), pk2(p[2], p[3]), pk2(p[4], p[5]), pk2(p[6], p[7])};
        *(u32x4*)(dst + lane * 16) = o;
        u32x2 o2;
        if (h == 0) o2 = u32x2{pk2(rb[r * 49 + 16], rb[r * 49 + 17]), 0x3f803f80u};
        else o2 = u32x2{0x00003f80u, 0u};
        *(u32x2*)(dst + 3072 + lane * 8) = o2;
      } else {
#pragma unroll
        for (int s = 0; s < 2; s++) {
          float v[8];
#pragma unroll
          for (int j = 0; j < 8; j++) {
            int key = 16 * s + 8 * (j >> 2) + 4 * h + (j & 3);
            float t = rb[key * 49 + 18 + (r < 18 ? r : 0)];
            v[j] = r < 18 ? t : ((r == 18 || r == 20) ? 1.0f : 0.0f);
          }
          u32x4 o = {pk2(v[0], v[1]), pk2(v[2], v[3]), pk2(v[4], v[5]), pk2(v[6], v[7])};
          *(u32x4*)(dst + 1024 + s * 1024 + lane * 16) = o;
        }
      }
    }
    return;
  }
  if (w >= 4) {
    const int lw = w - 4;
    const u16* W = ct < 12 ? w_in + FH + ct * 192 : wq + (ct - 12) * 192;
    const int ldw = ct < 12 ? 4608 : 576;
    const int r8 = lane >> 3, p = lane & 7;
    const u16* asrc[4];
    const u16* bsrc[6];
#pragma unroll
    for (int j = 0; j < 4; j++) {
      int row = (lw * 4 + j) * 8 + r8;
      int f = (row & 3) | (((row >> 3) & 1) << 2);
      asrc[j] = x + (size_t)(rt * 128 + row) * 576 + (p ^ f) * 8;
    }
#pragma unroll
    for (int j = 0; j < 6; j++) {
      int st = lw * 6 + j, ng = st >> 3, kb = st & 7;
      int h = ((r8 >> 1) & 1) | ((kb & 1) << 1);
      bsrc[j] = W + (size_t)(kb * 8 + r8) * ldw + ng * 64 + (p ^ (h << 1)) * 8;
    }
    auto issue = [&](int kt) {
      char* st = sm + (kt % K1_NS) * K1_ST;
#pragma unroll
      for (int j = 0; j < 4; j++) glds16(asrc[j] + kt * 64, st + (lw * 4 + j) * 1024);
#pragma unroll
      for (int j = 0; j < 6; j++) glds16(bsrc[j] + (size_t)kt * 64 * ldw, st + 16384 + (lw * 6 + j) * 1024);
    };
    issue(0); issue(1); issue(2);
    WAITV(20); BAR();
#pragma unroll
    for (int kt = 0; kt < 9; kt++) {
      if (kt < 7) WAITV(10); else WAITV(0);
      BAR();
      if (kt + 3 < 9) issue(kt + 3);
    }
    return;
  }
  if (ct < 12) k1_consumer<false>(sm, w, lane, rt, ct, ws);
  else k1_consumer<true>(sm, w, lane, rt, ct, ws);
}

__device__ __forceinline__ float wave_sum(float v) {
#pragma unroll
  for (int o = 32; o >= 1; o >>= 1) v += __shfl_xor(v, o, 64);
  return v;
}

__global__ __launch_bounds__(512, 2) void k2_attn(const u16* __restrict__ w_in, const u16* __restrict__ wo,
                                                  const u16* __restrict__ w_out, char* __restrict__ ws) {
  __shared__ __attribute__((aligned(16))) float part[2 * 256 * 20 + 512 + 256];
  const int tid = threadIdx.x, lane = tid & 63;
  const int w = __builtin_amdgcn_readfirstlane(tid >> 6);
  const int b = blockIdx.x;
  const int r = lane & 31, h = lane >> 5;
  const int pair = w & 3, ksp = w >> 2;
  char* zbase = ws + WS_Z2 + (size_t)(b >> 2) * 1024 + (b & 3) * 256 + (tid & 15) * 16;
  u32x4 zr[5];
#pragma unroll
  for (int j = 0; j < 5; j++) {
    int ft = j * 32 + (tid >> 4);
    zr[j] = u32x4{0, 0, 0, 0};
    if (ft < 144) zr[j] = *(const u32x4*)(zbase + (size_t)ft * 65536);
  }
  for (int fr = b * 8 + w; fr < 5832; fr += 2048) {
    const u16* src; int ld; char* dst;
    const int i = lane & 15, g = lane >> 4;
    if (fr < 2592) {
      int kt = fr / 144, ft = fr % 144;
      src = w_in + (size_t)(kt * 32 + g * 8) * 4608 + ft * 16 + i; ld = 4608;
      dst = ws + WS_W1 + (size_t)(ft * 18 + kt) * 1024;
    } else {
      int f2 = fr - 2592, kt = f2 / 36, nt = f2 % 36;
      src = (kt < 18 ? wo + (size_t)(kt * 32 + g * 8) * 576 : w_out + (size_t)((kt - 18) * 32 + g * 8) * 576) + nt * 16 + i;
      ld = 576;
      dst = ws + WS_WO + (size_t)(nt * 90 + kt) * 1024;
    }
    unsigned v[8];
#pragma unroll
    for (int j = 0; j < 8; j++) v[j] = src[(size_t)j * ld];
    *(u32x4*)(dst + lane * 16) = u32x4{v[0] | (v[1] << 16), v[2] | (v[3] << 16), v[4] | (v[5] << 16), v[6] | (v[7] << 16)};
  }
  const char* kv = ws + WS_KV + (size_t)ksp * 32 * 3584;
  bfx8 q0[2]; u32x2 q1[2]; unsigned qd[2];
#pragma unroll
  for (int a = 0; a < 2; a++) {
    const char* qp = ws + WS_Q + ((size_t)(b * 8 + pair * 2 + a) * 576 + r * 18) * 2;
    q0[a] = __builtin_bit_cast(bfx8, (u32x4) * (const u32x4_a4*)(qp + 16 * h));
    qd[a] = h == 0 ? *(const unsigned*)(qp + 32) : 0u;
  }
  f32x16 O[2];
  float mrow[2];
  const f32x16 zero16 = {0.f, 0.f, 0.f, 0.f, 0.f, 0.f, 0.f, 0.f, 0.f, 0.f, 0.f, 0.f, 0.f, 0.f, 0.f, 0.f};
  for (int attempt = 0; attempt < 2; attempt++) {
    const int nmax = attempt == 0 ? 1 : 32;
    mrow[0] = mrow[1] = -INFINITY;
    for (int t = 0; t < nmax; t++) {
      bfx8 k0 = *(const bfx8*)(kv + t * 3584 + lane * 16);
      s16x4 k1 = *(const s16x4*)(kv + t * 3584 + 3072 + lane * 8);
#pragma unroll
      for (int a = 0; a < 2; a++) {
        f32x16 s = MFMA32(k0, q0[a], zero16);
        s = MFMA32H(k1, __builtin_bit_cast(s16x4, u32x2{qd[a], 0u}), s);
#pragma unroll
        for (int j = 0; j < 16; j++) mrow[a] = fmaxf(mrow[a], s[j]);
      }
    }
#pragma unroll
    for (int a = 0; a < 2; a++) {
      float m = fmaxf(mrow[a], __shfl_xor(mrow[a], 32, 64));
      mrow[a] = m;
      float nm = -m;
      unsigned hi = __float_as_uint(nm) & 0xffff0000u;
      float r1 = nm - __uint_as_float(hi);
      unsigned mid = __float_as_uint(r1) & 0xffff0000u;
      float r2 = r1 - __uint_as_float(mid);
      unsigned lo = __float_as_uint(r2) & 0xffff0000u;
      q1[a] = h == 0 ? u32x2{qd[a], (hi >> 16) | mid} : u32x2{lo >> 16, 0u};
      O[a] = zero16;
    }
    bfx8 k0 = *(const bfx8*)(kv + lane * 16);
    s16x4 k1 = *(const s16x4*)(kv + 3072 + lane * 8);
    bfx8 v0 = *(const bfx8*)(kv + 1024 + lane * 16), v1 = *(const bfx8*)(kv + 2048 + lane * 16);
    f32x16 sA = MFMA32(k0, q0[0], zero16);
    sA = MFMA32H(k1, __builtin_bit_cast(s16x4, q1[0]), sA);
#pragma unroll 2
    for (int t = 0; t < 32; t++) {
      const char* nx = kv + (t < 31 ? t + 1 : t) * 3584;
      bfx8 k0n = *(const bfx8*)(nx + lane * 16);
      s16x4 k1n = *(const s16x4*)(nx + 3072 + lane * 8);
      bfx8 v0n = *(const bfx8*)(nx + 1024 + lane * 16), v1n = *(const bfx8*)(nx + 2048 + lane * 16);
      SCHED0();
      f32x16 sB = MFMA32(k0, q0[1], zero16);
      sB = MFMA32H(k1, __builtin_bit_cast(s16x4, q1[1]), sB);
      u32x4 pa, pb;
#pragma unroll
      for (int j = 0; j < 4; j++) pa[j] = pk2(__builtin_amdgcn_exp2f(sA[2 * j]), __builtin_amdgcn_exp2f(sA[2 * j + 1]));
      O[0] = MFMA32(v0, __builtin_bit_cast(bfx8, pa), O[0]);
#pragma unroll
      for (int j = 0; j < 4; j++) pb[j] = pk2(__builtin_amdgcn_exp2f(sA[8 + 2 * j]), __builtin_amdgcn_exp2f(sA[9 + 2 * j]));
      O[0] = MFMA32(v1, __builtin_bit_cast(bfx8, pb), O[0]);
      __builtin_amdgcn_sched_group_barrier(0x2, 3, 0);
      __builtin_amdgcn_sched_group_barrier(0x8, 1, 0);
      __builtin_amdgcn_sched_group_barrier(0x2, 5, 0);
      __builtin_amdgcn_sched_group_barrier(0x8, 1, 0);
      __builtin_amdgcn_sched_group_barrier(0x2, 6, 0);
      __builtin_amdgcn_sched_group_barrier(0x8, 1, 0);
      __builtin_amdgcn_sched_group_barrier(0x2, 10, 0);
      __builtin_amdgcn_sched_group_barrier(0x8, 1, 0);
      SCHED0();
      sA = MFMA32(k0n, q0[0], zero16);
      sA = MFMA32H(k1n, __builtin_bit_cast(s16x4, q1[0]), sA);
#pragma unroll
      for (int j = 0; j < 4; j++) pa[j] = pk2(__builtin_amdgcn_exp2f(sB[2 * j]), __builtin_amdgcn_exp2f(sB[2 * j + 1]));
      O[1] = MFMA32(v0, __builtin_bit_cast(bfx8, pa), O[1]);
#pragma unroll
      for (int j = 0; j < 4; j++) pb[j] = pk2(__builtin_amdgcn_exp2f(sB[8 + 2 * j]), __builtin_amdgcn_exp2f(sB[9 + 2 * j]));
      O[1] = MFMA32(v1, __builtin_bit_cast(bfx8, pb), O[1]);
      __builtin_amdgcn_sched_group_barrier(0x2, 3, 1);
      __builtin_amdgcn_sched_group_barrier(0x8, 1, 1);
      __builtin_amdgcn_sched_group_barrier(0x2, 5, 1);
      __builtin_amdgcn_sched_group_barrier(0x8, 1, 1);
      __builtin_amdgcn_sched_group_barrier(0x2, 6, 1);
      __builtin_amdgcn_sched_group_barrier(0x8, 1, 1);
      __builtin_amdgcn_sched_group_barrier(0x2, 10, 1);
      __builtin_amdgcn_sched_group_barrier(0x8, 1, 1);
      SCHED0();
      k0 = k0n; k1 = k1n; v0 = v0n; v1 = v1n;
    }
    float chk = 0.f;
#pragma unroll
    for (int a = 0; a < 2; a++)
#pragma unroll
      for (int j = 0; j < 11; j++) chk += fabsf(O[a][j]);
    float lmax = fmaxf(h == 0 ? O[0][10] : O[0][8], h == 0 ? O[1][10] : O[1][8]);
    bool bad = !(lmax < 1.8e19f) || !(chk < 3.0e38f);
    if (!__any(bad)) break;
  }
#pragma unroll
  for (int a = 0; a < 2; a++) {
    float* p = part + ((ksp * 8 + pair * 2 + a) * 32 + r) * 20;
    *(f32x4*)(p + 4 * h) = f32x4{O[a][0], O[a][1], O[a][2], O[a][3]};
    *(f32x4*)(p + 8 + 4 * h) = f32x4{O[a][4], O[a][5], O[a][6], O[a][7]};
    if (h == 0) *(f32x4*)(p + 16) = f32x4{O[a][8], O[a][9], O[a][10], mrow[a]};
  }
  float* sc = part + 2 * 256 * 20;
  float* red = sc + 512;
  {
    float s1[8], s2[8];
#pragma unroll
    for (int j = 0; j < 8; j++) s1[j] = s2[j] = 0.f;
#pragma unroll
    for (int c = 0; c < 5; c++)
#pragma unroll
      for (int j = 0; j < 4; j++) {
        float a = bflo(zr[c][j]), bb = bfhi(zr[c][j]);
        s1[2 * j] += a; s2[2 * j] = __builtin_fmaf(a, a, s2[2 * j]);
        s1[2 * j + 1] += bb; s2[2 * j + 1] = __builtin_fmaf(bb, bb, s2[2 * j + 1]);
      }
#pragma unroll
    for (int j = 0; j < 8; j++) { s1[j] = wave_sum(s1[j]); s2[j] = wave_sum(s2[j]); }
    if (lane == 0) {
#pragma unroll
      for (int j = 0; j < 8; j++) { red[w * 16 + j] = s1[j]; red[w * 16 + 8 + j] = s2[j]; }
    }
  }
  __syncthreads();
  if (tid < 256) {
    const float* p0 = part + tid * 20; const float* p1 = p0 + 256 * 20;
    float m0 = p0[19], m1 = p1[19], M = fmaxf(m0, m1);
    float a0 = __builtin_amdgcn_exp2f(m0 - M), a1 = __builtin_amdgcn_exp2f(m1 - M);
    float gt = ((const float*)(ws + WS_G))[b * 8 + (tid >> 5)];
    float inv = gt / (p0[18] * a0 + p1[18] * a1);
    sc[tid * 2] = a0 * inv; sc[tid * 2 + 1] = a1 * inv;
  }
  {
    float mu[8], rs[8];
#pragma unroll
    for (int j = 0; j < 8; j++) {
      float a = 0.f, c2 = 0.f;
#pragma unroll
      for (int ww = 0; ww < 8; ww++) { a += red[ww * 16 + j]; c2 += red[ww * 16 + 8 + j]; }
      float mean = a * (1.0f / 2304.0f);
      float var = fmaxf(c2 * (1.0f / 2304.0f) - mean * mean, 0.f);
      rs[j] = __builtin_amdgcn_rsqf(var + 1e-5f);
      mu[j] = -mean * rs[j];
    }
#pragma unroll
    for (int c = 0; c < 5; c++) {
      int ft = c * 32 + (tid >> 4);
      u32x4 o;
#pragma unroll
      for (int j = 0; j < 4; j++)
        o[j] = pk2(__builtin_fmaf(bflo(zr[c][j]), rs[2 * j], mu[2 * j]), __builtin_fmaf(bfhi(zr[c][j]), rs[2 * j + 1], mu[2 * j + 1]));
      if (ft < 144) *(u32x4*)(zbase + (size_t)ft * 65536) = o;
    }
  }
  __syncthreads();
  for (int c = tid; c < 576; c += 512) {
    int tl = c & 7, ch = c >> 3;
    float v[8];
#pragma unroll
    for (int j = 0; j < 8; j++) {
      int k = ch * 8 + j, hd = k / 18, d = k - hd * 18, row = tl * 32 + hd;
      v[j] = part[row * 20 + d] * sc[row * 2] + part[(256 + row) * 20 + d] * sc[row * 2 + 1];
    }
    int t = b * 8 + tl;
    char* dst = ws + WS_AH + ((size_t)(t >> 4) * 90 + (ch >> 2)) * 1024 + ((t & 15) + 16 * (ch & 3)) * 16;
    *(u32x4*)dst = u32x4{pk2(v[0], v[1]), pk2(v[2], v[3]), pk2(v[4], v[5]), pk2(v[6], v[7])};
  }
}

constexpr int K3_ST = 34816, K3_NS = 4;

__global__ __launch_bounds__(512, 2) void k3_spatial(const u16* __restrict__ x, const u16* __restrict__ w_sp,
                                                     const u16* __restrict__ b_sp, char* __restrict__ ws) {
  __shared__ __attribute__((aligned(1024))) char sm[K3_ST * K3_NS];
  const int tid = threadIdx.x, lane = tid & 63;
  const int w = __builtin_amdgcn_readfirstlane(tid >> 6);
  const int b = blockIdx.x, xcd = b & 7, ii = b >> 3;
  const int rt = (xcd & 1) * 8 + (ii & 7), ct = (xcd >> 1) * 4 + (ii >> 3);
  if (w >= 4) {
    const int lw = w - 4, r8 = lane >> 3, p = lane & 7;
    const int nb = lw < 2 ? 5 : 4, b0 = lw < 2 ? lw * 5 : 10 + (lw - 2) * 4;
    const char* a1 = (const char*)x + (size_t)(rt * 128 + lw * 32 + r8) * 1152 + (p ^ r8) * 16;
    const char* a2 = (const char*)w_sp + (size_t)(rt * 128 + lw * 32 + r8) * 4096 + (p ^ r8) * 16;
    const char* b1 = ws + WS_W1 + (size_t)ct * 9 * 18 * 1024 + lane * 16;
    const char* b2 = ws + WS_Z2 + (size_t)ct * 9 * 64 * 1024 + lane * 16;
    auto issue = [&](int j) {
      char* st = sm + (j & 3) * K3_ST;
      if (j < 9) {
#pragma unroll
        for (int u = 0; u < 4; u++) glds16(a1 + u * 8 * 1152 + j * 128, st + (lw * 4 + u) * 1024);
#pragma unroll
        for (int u = 0; u < 5; u++)
          if (u < nb) { int pc = b0 + u; glds16(b1 + ((pc >> 1) * 18 + 2 * j + (pc & 1)) * 1024, st + 16384 + pc * 1024); }
      } else {
#pragma unroll
        for (int u = 0; u < 4; u++) glds16(a2 + u * 8 * 4096 + (j - 9) * 128, st + (lw * 4 + u) * 1024);
#pragma unroll
        for (int u = 0; u < 5; u++)
          if (u < nb) { int pc = b0 + u; glds16(b2 + ((pc >> 1) * 64 + 2 * (j - 9) + (pc & 1)) * 1024, st + 16384 + pc * 1024); }
      }
    };
    issue(0); issue(1); issue(2); issue(3);
    if (lw < 2) WAITV(27); else WAITV(24);
    BAR();
    for (int j = 0; j < 38; j++) {
      if (lw < 2) WAITV(18); else WAITV(16);
      BAR();
      if (j + 4 < 41) issue(j + 4);
    }
    if (lw < 2) WAITV(9); else WAITV(8);
    BAR();
    WAITV(0); BAR();
    BAR();
    return;
  }
  const int i = lane & 15, g = lane >> 4;
  f32x4 acc[9][2];
  unsigned z1p[9][2][2];
#pragma unroll
  for (int n = 0; n < 9; n++) acc[n][0] = acc[n][1] = f32x4{0.f, 0.f, 0.f, 0.f};
  const int aoff0 = (w * 32 + i) * 128 + ((g ^ (i & 7)) * 16), aoff1 = aoff0 ^ 64;
  const int boff = 16384 + lane * 16;
  bfx8 Af[2][2], Bf[2][9];
  auto rd = [&](int buf, char* st, int ks) {
#pragma unroll
    for (int m = 0; m < 2; m++) Af[buf][m] = *(const bfx8*)(st + (ks ? aoff1 : aoff0) + m * 2048);
#pragma unroll
    for (int n = 0; n < 9; n++) Bf[buf][n] = *(const bfx8*)(st + boff + (n * 2 + ks) * 1024);
  };
  auto mm = [&](int buf) {
#pragma unroll
    for (int n = 0; n < 9; n++)
#pragma unroll
      for (int m = 0; m < 2; m++) acc[n][m] = MFMA16(Bf[buf][n], Af[buf][m], acc[n][m]);
  };
  auto ilv = [&]() {
#pragma unroll
    for (int x2 = 0; x2 < 11; x2++) {
      __builtin_amdgcn_sched_group_barrier(0x8, 1, 0);
      __builtin_amdgcn_sched_group_barrier(0x100, 1, 0);
    }
    __builtin_amdgcn_sched_group_barrier(0x8, 7, 0);
  };
  auto body = [&](int j) {
    char* st = sm + (j & 3) * K3_ST;
    rd(1, st, 1); mm(0); ilv();
    SCHED0(); WAITL0(); BAR(); SCHED0();
    rd(0, sm + ((j + 1) & 3) * K3_ST, 0);
    mm(1); ilv();
    SCHED0();
  };
  BAR(); SCHED0();
  rd(0, sm, 0);
  SCHED0();
  for (int j = 0; j < 9; j++) body(j);
#pragma unroll
  for (int n = 0; n < 9; n++)
#pragma unroll
    for (int m = 0; m < 2; m++) {
      f32x4 a = acc[n][m];
      z1p[n][m][0] = pk2(gelu_t(a[0]), gelu_t(a[1]));
      z1p[n][m][1] = pk2(gelu_t(a[2]), gelu_t(a[3]));
      acc[n][m] = f32x4{0.f, 0.f, 0.f, 0.f};
    }
  SCHED0();
  for (int j = 9; j < 41; j++) body(j);
#pragma unroll
  for (int m = 0; m < 2; m++) {
    const int t = rt * 128 + w * 32 + m * 16 + i;
    const float og = 1.0f - ((const float*)(ws + WS_G))[t];
    const float bs = __uint_as_float((unsigned)b_sp[t] << 16);
    char* dst = ws + WS_AH + (size_t)(t >> 4) * 90 * 1024 + i * 16 + (g & 1) * 8;
#pragma unroll
    for (int n = 0; n < 9; n++) {
      f32x4 a = acc[n][m];
      int f0 = ct * 144 + n * 16;
      u32x2 o = {pk2(og * bflo(z1p[n][m][0]) * (a[0] + bs), og * bfhi(z1p[n][m][0]) * (a[1] + bs)),
                 pk2(og * bflo(z1p[n][m][1]) * (a[2] + bs), og * bfhi(z1p[n][m][1]) * (a[3] + bs))};
      *(u32x2*)(dst + (18 + (f0 >> 5)) * 1024 + (((f0 & 31) >> 3) + (g >> 1)) * 256) = o;
    }
  }
}

template <int NT>
__device__ __forceinline__ void k4_body(float* red, const char* ws, u16* out, int rt, int nt0, int w, int lane) {
  const int kt0 = w < 2 ? w * 23 : 46 + (w - 2) * 22, nk = w < 2 ? 23 : 22;
  const char* ap = ws + WS_AH + ((size_t)rt * 4 * 90 + kt0) * 1024 + lane * 16;
  const char* bp = ws + WS_WO + ((size_t)nt0 * 90 + kt0) * 1024 + lane * 16;
  f32x4 acc[4][NT];
#pragma unroll
  for (int m = 0; m < 4; m++)
#pragma unroll
    for (int n = 0; n < NT; n++) acc[m][n] = f32x4{0.f, 0.f, 0.f, 0.f};
  bfx8 Af[3][4], Bf[3][NT];
  auto ld = [&](int buf, int k) {
    int kk = k < nk ? k : nk - 1;
#pragma unroll
    for (int m = 0; m < 4; m++) Af[buf][m] = *(const bfx8*)(ap + (size_t)(m * 90 + kk) * 1024);
#pragma unroll
    for (int n = 0; n < NT; n++) Bf[buf][n] = *(const bfx8*)(bp + (size_t)(n * 90 + kk) * 1024);
  };
  auto mm = [&](int buf) {
#pragma unroll
    for (int m = 0; m < 4; m++)
#pragma unroll
      for (int n = 0; n < NT; n++) acc[m][n] = MFMA16(Bf[buf][n], Af[buf][m], acc[m][n]);
  };
  ld(0, 0); ld(1, 1); ld(2, 2);
  for (int k = 0; k < 21; k += 3) {
    SCHED0(); mm(0); SCHED0(); ld(0, k + 3);
    SCHED0(); mm(1); SCHED0(); ld(1, k + 4);
    SCHED0(); mm(2); SCHED0(); if (k < 18) ld(2, k + 5);
  }
  SCHED0();
  mm(0);
  if (nk == 23) mm(1);
  constexpr int LD = NT * 16 + 4;
  const int i = lane & 15, g = lane >> 4;
#pragma unroll
  for (int m = 0; m < 4; m++)
#pragma unroll
    for (int n = 0; n < NT; n++) *(f32x4*)(red + (size_t)(w * 64 + m * 16 + i) * LD + n * 16 + g * 4) = acc[m][n];
  __syncthreads();
  const int tid = w * 64 + lane;
  for (int c = tid; c < 64 * NT * 2; c += 256) {
    int row = c / (NT * 2), ch = c % (NT * 2);
    const float* p = red + row * LD + ch * 8;
    f32x4 s0 = *(const f32x4*)p, s1 = *(const f32x4*)(p + 4);
#pragma unroll
    for (int ww = 1; ww < 4; ww++) { s0 += *(const f32x4*)(p + ww * 64 * LD); s1 += *(const f32x4*)(p + ww * 64 * LD + 4); }
    *(u32x4*)(out + (size_t)(rt * 64 + row) * 576 + nt0 * 16 + ch * 8) =
        u32x4{pk2(s0[0], s0[1]), pk2(s0[2], s0[3]), pk2(s1[0], s1[1]), pk2(s1[2], s1[3])};
  }
}

__global__ __launch_bounds__(256, 2) void k4_out(const char* __restrict__ ws, u16* __restrict__ out) {
  __shared__ __attribute__((aligned(16))) float red[4 * 64 * 84];
  const int lane = threadIdx.x & 63;
  const int w = __builtin_amdgcn_readfirstlane(threadIdx.x >> 6);
  const int b = blockIdx.x, xcd = b & 7, ii = b >> 3;
  const int rt = xcd * 4 + (ii & 3), cg = ii >> 2;
  const int nt0 = (cg >> 1) * 9 + (cg & 1) * 5;
  if (cg & 1) k4_body<4>(red, ws, out, rt, nt0, w, lane);
  else k4_body<5>(red, ws, out, rt, nt0, w, lane);
}

extern "C" void kernel_launch(void* const* d_in, const int* in_sizes, int n_in, void* d_out, int out_size,
                              void* d_ws, size_t ws_size, hipStream_t stream) {
  const u16* x = (const u16*)d_in[0];
  const u16* wq = (const u16*)d_in[1];
  const u16* wk = (const u16*)d_in[2];
  const u16* wv = (const u16*)d_in[3];
  const u16* wo = (const u16*)d_in[4];
  const u16* w_in = (const u16*)d_in[6];
  const u16* w_sp = (const u16*)d_in[7];
  const u16* b_sp = (const u16*)d_in[8];
  const u16* w_out = (const u16*)d_in[9];
  const u16* gw = (const u16*)d_in[10];
  char* ws = (char*)d_ws;
  k1_proj<<<256, 512, 0, stream>>>(x, wq, wk, wv, w_in, gw, ws);
  k2_attn<<<256, 512, 0, stream>>>(w_in, wo, w_out, ws);
  k3_spatial<<<256, 512, 0, stream>>>(x, w_sp, b_sp, ws);
  k4_out<<<256, 256, 0, stream>>>(ws, (u16*)d_out);
}
```

```cpp
#include <hip/hip_runtime.h>
#include <stdint.h>

typedef __attribute__((ext_vector_type(8))) __bf16 bfx8;
typedef __attribute__((ext_vector_type(2))) __bf16 bfx2;
typedef __attribute__((ext_vector_type(4))) short s16x4;
typedef __attribute__((ext_vector_type(4))) float f32x4;
typedef __attribute__((ext_vector_type(16))) float f32x16;
typedef __attribute__((ext_vector_type(4))) unsigned u32x4;
typedef __attribute__((ext_vector_type(2))) unsigned u32x2;
typedef u32x4 __attribute__((aligned(4))) u32x4_a4;
typedef unsigned short u16;
#define LDSP __attribute__((address_space(3)))
#define GLBP __attribute__((address_space(1)))

constexpr int S_ = 2048, D_ = 576, FH = 2304;
constexpr size_t WS_Q   = 0;
constexpr size_t WS_KV  = WS_Q + (size_t)S_ * D_ * 2;
constexpr size_t WS_G   = WS_KV + 64 * 3584;
constexpr size_t WS_Z2  = WS_G + 2048 * 4;
constexpr size_t WS_W1  = WS_Z2 + (size_t)144 * 64 * 1024;
constexpr size_t WS_WO  = WS_W1 + (size_t)144 * 18 * 1024;
constexpr size_t WS_AH  = WS_WO + (size_t)36 * 90 * 1024;

__device__ __forceinline__ unsigned pk2(float a, float b) { bfx2 r; r[0] = (__bf16)a; r[1] = (__bf16)b; return __builtin_bit_cast(unsigned, r); }
__device__ __forceinline__ float bflo(unsigned u) { return __uint_as_float(u << 16); }
__device__ __forceinline__ float bfhi(unsigned u) { return __uint_as_float(u & 0xffff0000u); }
__device__ __forceinline__ float gelu_t(float x) {
  const float c0 = -2.302208198f;
  const float c1 = c0 * 0.044715f;
  float e = __builtin_amdgcn_exp2f(x * __builtin_fmaf(x * x, c1, c0));
  return x * __builtin_amdgcn_rcpf(1.0f + e);
}
__device__ __forceinline__ void glds16(const void* g, char* l) {
  __builtin_amdgcn_global_load_lds((const GLBP void*)g, (LDSP void*)l, 16, 0, 0);
}
#define MFMA16(a, b, c) __builtin_amdgcn_mfma_f32_16x16x32_bf16(a, b, c, 0, 0, 0)
#define MFMA32(a, b, c) __builtin_amdgcn_mfma_f32_32x32x16_bf16(a, b, c, 0, 0, 0)
#define MFMA32H(a, b, c) __builtin_amdgcn_mfma_f32_32x32x8bf16_1k(a, b, c, 0, 0, 0)
#define WAITV(n) asm volatile("s_waitcnt vmcnt(" #n ")" ::: "memory")
#define WAITL0() asm volatile("s_waitcnt lgkmcnt(0)" ::: "memory")
#define BAR() __builtin_amdgcn_s_barrier()
#define SCHED0() __builtin_amdgcn_sched_barrier(0)
#define STAMP0()
#define STAMP(k, i)
#define STAMPE(k, n)

constexpr int K1_ST = 40960, K1_NS = 3;

template <bool ISQ>
__device__ __forceinline__ void k1_consumer(char* sm, int w, int lane, int rt, int ct, char* ws) {
  const int wm = w & 1, wn = w >> 1, i = lane & 15, g = lane >> 4;
  f32x4 acc[4][6];
#pragma unroll
  for (int m = 0; m < 4; m++)
#pragma unroll
    for (int n = 0; n < 6; n++) acc[m][n] = f32x4{0.f, 0.f, 0.f, 0.f};
  const int arow = wm * 64 + 8 * (i >> 2) + (i & 3);
  const int aoff0 = arow * 128 + ((g ^ (i & 7)) * 16), aoff1 = aoff0 ^ 64;
  int boff[6];
  {
    const int q = i >> 2, p = i & 3, h = ((q >> 1) & 1) | ((g & 1) << 1);
#pragma unroll
    for (int n = 0; n < 6; n++) {
      int nt = wn * 6 + n;
      boff[n] = 16384 + (nt >> 2) * 8192 + g * 1024 + q * 128 + (((nt & 3) ^ h) * 32) + p * 8;
    }
  }
  bfx8 Af[2][4], Bf[2][6];
  auto rd = [&](int buf, char* st, int ks) {
#pragma unroll
    for (int n = 0; n < 6; n++) {
      s16x4 lo = __builtin_amdgcn_ds_read_tr16_b64_v4i16((LDSP s16x4*)(st + boff[n] + ks * 4096));
      s16x4 hi = __builtin_amdgcn_ds_read_tr16_b64_v4i16((LDSP s16x4*)(st + boff[n] + ks * 4096 + 512));
      Bf[buf][n] = __builtin_bit_cast(bfx8, __builtin_shufflevector(lo, hi, 0, 1, 2, 3, 4, 5, 6, 7));
    }
#pragma unroll
    for (int m = 0; m < 4; m++)
      Af[buf][m] = *(const bfx8*)(st + (ks ? aoff1 : aoff0) + (4 * (m & 1) + 32 * (m >> 1)) * 128);
  };
  auto mm = [&](int buf) {
#pragma unroll
    for (int m = 0; m < 4; m++)
#pragma unroll
      for (int n = 0; n < 6; n++)
        acc[m][n] = ISQ ? MFMA16(Bf[buf][n], Af[buf][m], acc[m][n]) : MFMA16(Af[buf][m], Bf[buf][n], acc[m][n]);
  };
  auto ilv = [&]() {
#pragma unroll
    for (int x = 0; x < 8; x++) {
      __builtin_amdgcn_sched_group_barrier(0x8, 2, 0);
      __builtin_amdgcn_sched_group_barrier(0x100, 2, 0);
    }
    __builtin_amdgcn_sched_group_barrier(0x8, 8, 0);
  };
  STAMP0(); STAMP(0, 0);
  BAR(); SCHED0();
  STAMP(0, 1);
  rd(0, sm, 0);
  SCHED0();
#pragma unroll
  for (int kt = 0; kt < 9; kt++) {
    char* st = sm + (kt % K1_NS) * K1_ST;
    rd(1, st, 1); mm(0); ilv();
    SCHED0(); WAITL0(); BAR(); SCHED0();
    if (kt < 8) rd(0, sm + ((kt + 1) % K1_NS) * K1_ST, 0);
    mm(1);
    if (kt < 8) ilv();
    SCHED0();
  }
  STAMP(0, 2);
  if (!ISQ) {
    char* z2 = ws + WS_Z2;
#pragma unroll
    for (int n = 0; n < 6; n++)
#pragma unroll
      for (int mp = 0; mp < 2; mp++) {
        f32x4 a = acc[2 * mp][n], b = acc[2 * mp + 1][n];
        u32x4 o = {pk2(gelu_t(a[0]), gelu_t(a[1])), pk2(gelu_t(a[2]), gelu_t(a[3])),
                   pk2(gelu_t(b[0]), gelu_t(b[1])), pk2(gelu_t(b[2]), gelu_t(b[3]))};
        int ft = ct * 12 + wn * 6 + n, kt32 = rt * 4 + wm * 2 + mp;
        *(u32x4*)(z2 + ((size_t)(ft * 64 + kt32) * 64 + lane) * 16) = o;
      }
  } else {
    const float c = 0.34004649f;
    char* q = ws + WS_Q;
#pragma unroll
    for (int m = 0; m < 4; m++) {
      int t = rt * 128 + arow + 4 * (m & 1) + 32 * (m >> 1);
#pragma unroll
      for (int n = 0; n < 6; n++) {
        f32x4 a = acc[m][n];
        u32x2 o = {pk2(a[0] * c, a[1] * c), pk2(a[2] * c, a[3] * c)};
        *(u32x2*)(q + ((size_t)t * 576 + (ct - 12) * 192 + wn * 96 + n * 16 + g * 4) * 2) = o;
      }
    }
  }
  STAMPE(0, 3);
}

__global__ __launch_bounds__(512, 2) void k1_proj(const u16* __restrict__ x, const u16* __restrict__ wq,
                                                  const u16* __restrict__ wk, const u16* __restrict__ wv,
                                                  const u16* __restrict__ w_in, const u16* __restrict__ gw,
                                                  char* __restrict__ ws) {
  __shared__ __attribute__((aligned(1024))) char sm[K1_ST * K1_NS];
  const int tid = threadIdx.x, lane = tid & 63;
  const int w = __builtin_amdgcn_readfirstlane(tid >> 6);
  const int b = blockIdx.x, xcd = b & 7, ii = b >> 3;
  const int rt = (xcd & 1) * 8 + (ii & 7), ct = (xcd >> 1) * 4 + (ii >> 3);
  if (ct == 15) {
    const int i = lane & 15, g = lane >> 4;
    bfx8 Af[18];
    const u16* xr = x + (size_t)(rt * 128 + w * 16 + i) * 576 + g * 8;
#pragma unroll
    for (int k = 0; k < 18; k++) Af[k] = *(const bfx8*)(xr + k * 32);
    for (int fr = w; fr < 54; fr += 8) {
      int nt = fr / 18, kt = fr % 18, n = nt * 16 + i, k0 = kt * 32 + g * 8;
      const u16* src = n < 18 ? wk + n : (n < 36 ? wv + (n - 18) : gw);
      int str = n < 36 ? 18 : 1;
      unsigned v[8];
#pragma unroll
      for (int j = 0; j < 8; j++) v[j] = src[(k0 + j) * str];
      u32x4 o = {v[0] | (v[1] << 16), v[2] | (v[3] << 16), v[4] | (v[5] << 16), v[6] | (v[7] << 16)};
      if (n > 36) o = u32x4{0, 0, 0, 0};
      *(u32x4*)(sm + fr * 1024 + lane * 16) = o;
    }
    __syncthreads();
    f32x4 acc[3] = {{0.f, 0.f, 0.f, 0.f}, {0.f, 0.f, 0.f, 0.f}, {0.f, 0.f, 0.f, 0.f}};
#pragma unroll
    for (int k = 0; k < 18; k++)
#pragma unroll
      for (int n = 0; n < 3; n++) acc[n] = MFMA16(Af[k], *(const bfx8*)(sm + (n * 18 + k) * 1024 + lane * 16), acc[n]);
    float* res = (float*)(sm + 56 * 1024);
#pragma unroll
    for (int n = 0; n < 3; n++)
#pragma unroll
      for (int r = 0; r < 4; r++) res[(w * 16 + g * 4 + r) * 49 + n * 16 + i] = acc[n][r];
    __syncthreads();
    char* kv = ws + WS_KV + (size_t)rt * 4 * 3584;
    if (tid < 128) {
      float s = res[tid * 49 + 36];
      ((float*)(ws + WS_G))[rt * 128 + tid] = __builtin_amdgcn_rcpf(1.0f + __builtin_amdgcn_exp2f(-1.44269504f * s));
    }
    {
      const int tile = w >> 1, r = lane & 31, h = lane >> 5;
      const float* rb = res + tile * 32 * 49;
      char* dst = kv + tile * 3584;
      if ((w & 1) == 0) {
        const float* p = rb + r * 49 + 8 * h;
        u32x4 o = {pk2(p[0], p[1]), pk2(p[2], p[3]), pk2(p[4], p[5]), pk2(p[6], p[7])};
        *(u32x4*)(dst + lane * 16) = o;
        u32x2 o2;
        if (h == 0) o2 = u32x2{pk2(rb[r * 49 + 16], rb[r * 49 + 17]), 0x3f803f80u};
        else o2 = u32x2{0x00003f80u, 0u};
        *(u32x2*)(dst + 3072 + lane * 8) = o2;
      } else {
#pragma unroll
        for (int s = 0; s < 2; s++) {
          float v[8];
#pragma unroll
          for (int j = 0; j < 8; j++) {
            int key = 16 * s + 8 * (j >> 2) + 4 * h + (j & 3);
            float t = rb[key * 49 + 18 + (r < 18 ? r : 0)];
            v[j] = r < 18 ? t : ((r == 18 || r == 20) ? 1.0f : 0.0f);
          }
          u32x4 o = {pk2(v[0], v[1]), pk2(v[2], v[3]), pk2(v[4], v[5]), pk2(v[6], v[7])};
          *(u32x4*)(dst + 1024 + s * 1024 + lane * 16) = o;
        }
      }
    }
    return;
  }
  if (w >= 4) {
    const int lw = w - 4;
    const u16* W = ct < 12 ? w_in + FH + ct * 192 : wq + (ct - 12) * 192;
    const int ldw = ct < 12 ? 4608 : 576;
    const int r8 = lane >> 3, p = lane & 7;
    const u16* asrc[4];
    const u16* bsrc[6];
#pragma unroll
    for (int j = 0; j < 4; j++) {
      int row = (lw * 4 + j) * 8 + r8;
      int f = (row & 3) | (((row >> 3) & 1) << 2);
      asrc[j] = x + (size_t)(rt * 128 + row) * 576 + (p ^ f) * 8;
    }
#pragma unroll
    for (int j = 0; j < 6; j++) {
      int st = lw * 6 + j, ng = st >> 3, kb = st & 7;
      int h = ((r8 >> 1) & 1) | ((kb & 1) << 1);
      bsrc[j] = W + (size_t)(kb * 8 + r8) * ldw + ng * 64 + (p ^ (h << 1)) * 8;
    }
    auto issue = [&](int kt) {
      char* st = sm + (kt % K1_NS) * K1_ST;
#pragma unroll
      for (int j = 0; j < 4; j++) glds16(asrc[j] + kt * 64, st + (lw * 4 + j) * 1024);
#pragma unroll
      for (int j = 0; j < 6; j++) glds16(bsrc[j] + (size_t)kt * 64 * ldw, st + 16384 + (lw * 6 + j) * 1024);
    };
    issue(0); issue(1); issue(2);
    WAITV(20); BAR();
#pragma unroll
    for (int kt = 0; kt < 9; kt++) {
      if (kt < 7) WAITV(10); else WAITV(0);
      BAR();
      if (kt + 3 < 9) issue(kt + 3);
    }
    return;
  }
  if (ct < 12) k1_consumer<false>(sm, w, lane, rt, ct, ws);
  else k1_consumer<true>(sm, w, lane, rt, ct, ws);
}

#define BPRIO 1
template <bool B> struct BoolC { static constexpr bool value = B; };
template <int CTRL>
__device__ __forceinline__ float dpp_add(float v) {
  return v + __builtin_bit_cast(float, __builtin_amdgcn_update_dpp(0, __builtin_bit_cast(int, v), CTRL, 0xf, 0xf, true));
}
#define POLL4(c) do { while (__hip_atomic_load(&(c), __ATOMIC_RELAXED, __HIP_MEMORY_SCOPE_WORKGROUP) < 4u) __builtin_amdgcn_s_sleep(1); asm volatile("" ::: "memory"); } while (0)
#define PIN(x) asm volatile("" : "+v"(x))
#define EX2(x) __builtin_amdgcn_exp2f(x)
#define ATT_STEP(sc, sn, K0, K1, Q0, Q1, Ocur, V0, Oprev, V1p, pprev, pcur)                          \
  {                                                                                                  \
    u32x4 pa;                                                                                        \
    sn = MFMA32(K0, Q0, zero16); PIN(sn); SCHED0();                                                  \
    { float e0 = EX2(sc[0]), e1 = EX2(sc[1]), e2 = EX2(sc[2]), e3 = EX2(sc[3]);                      \
      pa[0] = pk2(e0, e1); pa[1] = pk2(e2, e3); } PIN(pa); SCHED0();                                  \
    sn = MFMA32H(K1, __builtin_bit_cast(s16x4, Q1), sn); PIN(sn); SCHED0();                          \
    { float e0 = EX2(sc[4]), e1 = EX2(sc[5]), e2 = EX2(sc[6]), e3 = EX2(sc[7]);                      \
      pa[2] = pk2(e0, e1); pa[3] = pk2(e2, e3); } PIN(pa); SCHED0();                                  \
    Oprev = MFMA32(V1p, __builtin_bit_cast(bfx8, pprev), Oprev); PIN(Oprev); SCHED0();               \
    { float e0 = EX2(sc[8]), e1 = EX2(sc[9]), e2 = EX2(sc[10]), e3 = EX2(sc[11]);                    \
      pcur[0] = pk2(e0, e1); pcur[1] = pk2(e2, e3); } PIN(pcur); SCHED0();                           \
    Ocur = MFMA32(V0, __builtin_bit_cast(bfx8, pa), Ocur); PIN(Ocur); SCHED0();                      \
    { float e0 = EX2(sc[12]), e1 = EX2(sc[13]), e2 = EX2(sc[14]), e3 = EX2(sc[15]);                  \
      pcur[2] = pk2(e0, e1); pcur[3] = pk2(e2, e3); } PIN(pcur); SCHED0();                           \
  }

__global__ __launch_bounds__(512, 2) void k2_attn(const u16* __restrict__ w_in, const u16* __restrict__ wo,
                                                  const u16* __restrict__ w_out, const u16* __restrict__ w_sp,
                                                  char* __restrict__ ws) {
  __shared__ __attribute__((aligned(1024))) char kvs[32 * 3584];
  float* part = (float*)kvs;
  __shared__ __attribute__((aligned(16))) float red[16 * 16];
  __shared__ unsigned cnt[4];
  __shared__ __attribute__((aligned(16))) unsigned obuf[8 * 288];
  const int tid = threadIdx.x, lane = tid & 63;
  const int w = __builtin_amdgcn_readfirstlane(tid >> 6);
  const int b = blockIdx.x;
  const int r = lane & 31, h = lane >> 5;
  const int qh = w & 1, ksp = w >> 1;
  STAMP0(); STAMP(1, 0);
  const unsigned l16 = lane * 16, l8 = lane * 8;
  if (tid < 3) cnt[tid] = 0u;
  WAITL0(); BAR();
  if (w >= 4) {
    const int pc0 = (w & 1) * 56 + ((w >> 1) & 1);
#pragma unroll
    for (int j = 0; j < 28; j++) glds16(ws + WS_KV + (pc0 + 2 * j) * 1024 + l16, kvs + (pc0 + 2 * j) * 1024);
  }
  if (w >= 4) {
    const int hw = w - 4, i = lane & 15, g = lane >> 4;
    __builtin_amdgcn_s_setprio(3);
    WAITV(21);
    if (lane == 0) asm volatile("ds_add_u32 %0, %1" ::"v"((unsigned)(uintptr_t)(LDSP unsigned*)&cnt[1]), "v"(1u) : "memory");
    WAITV(0);
    if (lane == 0) __hip_atomic_fetch_add(&cnt[2], 1u, __ATOMIC_RELAXED, __HIP_MEMORY_SCOPE_WORKGROUP);
    char* zu = ws + WS_Z2 + (size_t)b * 256 + (size_t)hw * 36 * 65536;
    const unsigned zo = i * 16 + g * 65536;
    u32x4 zr[9];
#pragma unroll
    for (int c = 0; c < 9; c++) zr[c] = *(const u32x4*)(zu + c * 4 * 65536 + zo);
    u32x2 wv_[2][8];
    char* wdst[2];
    const int job0 = hw * 256 + b;
#pragma unroll
    for (int u = 0; u < 2; u++) {
      const int job = u == 0 ? job0 : (job0 < 434 ? job0 + 1024 : job0);
      const u16* wsrc; int wld;
      if (job < 648) {
        int kt = job / 36, cb = job % 36;
        wsrc = w_in + (size_t)(kt * 32 + g * 8) * 4608 + cb * 64 + i * 4; wld = 4608;
        wdst[u] = ws + WS_W1 + (size_t)((cb * 4 + (i >> 2)) * 18 + kt) * 1024;
      } else {
        int j2 = job - 648, kt = j2 / 9, cb = j2 % 9;
        wsrc = (kt < 18 ? wo + (size_t)(kt * 32 + g * 8) * 576 : w_out + (size_t)((kt - 18) * 32 + g * 8) * 576) + cb * 64 + i * 4;
        wld = 576;
        wdst[u] = ws + WS_WO + (size_t)((cb * 4 + (i >> 2)) * 90 + kt) * 1024;
      }
      wdst[u] += ((i & 3) * 4 + g * 16) * 16;
#pragma unroll
      for (int j = 0; j < 8; j++) wv_[u][j] = *(const u32x2*)(wsrc + (size_t)j * wld);
    }
    const char* pf = (const char*)w_sp + (size_t)b * 32768 + (tid - 256) * 64;
    const unsigned pf0 = *(const unsigned*)pf, pf1 = *(const unsigned*)(pf + 16384);
    SCHED0();
    float s1[8], s2[8];
#pragma unroll
    for (int j = 0; j < 8; j++) s1[j] = s2[j] = 0.f;
#pragma unroll
    for (int c = 0; c < 9; c++)
#pragma unroll
      for (int j = 0; j < 4; j++) {
        float a = bflo(zr[c][j]), bb = bfhi(zr[c][j]);
        s1[2 * j] += a; s2[2 * j] = __builtin_fmaf(a, a, s2[2 * j]);
        s1[2 * j + 1] += bb; s2[2 * j + 1] = __builtin_fmaf(bb, bb, s2[2 * j + 1]);
      }
#pragma unroll
    for (int j = 0; j < 8; j++) {
      s1[j] = dpp_add<0x121>(dpp_add<0x122>(dpp_add<0x124>(dpp_add<0x128>(s1[j]))));
      s2[j] = dpp_add<0x121>(dpp_add<0x122>(dpp_add<0x124>(dpp_add<0x128>(s2[j]))));
    }
    if (i == 0) {
      float* p = red + (hw * 4 + g) * 16;
      *(f32x4*)p = f32x4{s1[0], s1[1], s1[2], s1[3]}; *(f32x4*)(p + 4) = f32x4{s1[4], s1[5], s1[6], s1[7]};
      *(f32x4*)(p + 8) = f32x4{s2[0], s2[1], s2[2], s2[3]}; *(f32x4*)(p + 12) = f32x4{s2[4], s2[5], s2[6], s2[7]};
    }
    WAITL0();
    if (lane == 0) __hip_atomic_fetch_add(&cnt[0], 1u, __ATOMIC_RELAXED, __HIP_MEMORY_SCOPE_WORKGROUP);
#pragma unroll
    for (int u = 0; u < 2; u++) {
#pragma unroll
      for (int j = 0; j < 8; j++) PIN(wv_[u][j]);
      if (u == 0 || job0 < 434) {
#pragma unroll
        for (int c = 0; c < 2; c++) {
          u32x4 lo, hi;
#pragma unroll
          for (int j = 0; j < 4; j++) {
            lo[j] = __builtin_amdgcn_perm(wv_[u][2 * j + 1][c], wv_[u][2 * j][c], 0x05040100u);
            hi[j] = __builtin_amdgcn_perm(wv_[u][2 * j + 1][c], wv_[u][2 * j][c], 0x07060302u);
          }
          *(u32x4*)(wdst[u] + c * 32) = lo; *(u32x4*)(wdst[u] + c * 32 + 16) = hi;
        }
      }
    }
    asm volatile("" ::"v"(pf0), "v"(pf1));
    POLL4(cnt[0]);
    float t = 0.f;
#pragma unroll
    for (int j = 0; j < 4; j++) t += red[j * 64 + lane];
    t += __shfl_xor(t, 16, 64); t += __shfl_xor(t, 32, 64);
    float t2 = __builtin_bit_cast(float, __builtin_amdgcn_update_dpp(0, __builtin_bit_cast(int, t), 0x128, 0xf, 0xf, true));
    float mean = t * (1.0f / 2304.0f);
    float var = fmaxf(t2 * (1.0f / 2304.0f) - mean * mean, 0.f);
    float rsv = __builtin_amdgcn_rsqf(var + 1e-5f), muv = -mean * rsv;
    float rs[8], mu[8];
#pragma unroll
    for (int j = 0; j < 8; j++) {
      rs[j] = __builtin_bit_cast(float, __builtin_amdgcn_readlane(__builtin_bit_cast(int, rsv), j));
      mu[j] = __builtin_bit_cast(float, __builtin_amdgcn_readlane(__builtin_bit_cast(int, muv), j));
    }
#pragma unroll
    for (int c = 0; c < 9; c++) {
      u32x4 o;
#pragma unroll
      for (int j = 0; j < 4; j++)
        o[j] = pk2(__builtin_fmaf(bflo(zr[c][j]), rs[2 * j], mu[2 * j]), __builtin_fmaf(bfhi(zr[c][j]), rs[2 * j + 1], mu[2 * j + 1]));
      *(u32x4*)(zu + c * 4 * 65536 + zo) = o;
    }
    SCHED0();
  }
  STAMP(1, 1);
  const char* kvu = ws + WS_KV + (size_t)ksp * 16 * 3584;
#define KV0(t, o) (*(const bfx8*)(kvu + ((t) * 3584 + (o)) + l16))
#define KV1(t) (*(const s16x4*)(kvu + ((t) * 3584 + 3072) + l8))
  bfx8 q0[4]; u32x2 q1[4]; unsigned qd[4];
#pragma unroll
  for (int a = 0; a < 4; a++) {
    const char* qp = ws + WS_Q + ((size_t)(b * 8 + qh * 4 + a) * 576 + r * 18) * 2;
    q0[a] = __builtin_bit_cast(bfx8, (u32x4) * (const u32x4_a4*)(qp + 16 * h));
    qd[a] = *(const unsigned*)(qp + 32);
  }
  bfx8 kA = KV0(0, 0);
  s16x4 kA1 = KV1(0);
  bfx8 vA0, vA1;
  f32x16 O[4], sA, sB;
  float mrow[4];
  u32x4 pX, pY;
  bfx8 kB, vB0, vB1;
  s16x4 kB1;
  const f32x16 zero16 = {0.f, 0.f, 0.f, 0.f, 0.f, 0.f, 0.f, 0.f, 0.f, 0.f, 0.f, 0.f, 0.f, 0.f, 0.f, 0.f};
#pragma unroll
  for (int a = 0; a < 4; a++) qd[a] = h == 0 ? qd[a] : 0u;
#pragma unroll
  for (int a = 0; a < 4; a++) {
    f32x16 s = MFMA32(kA, q0[a], zero16);
    s = MFMA32H(kA1, __builtin_bit_cast(s16x4, u32x2{qd[a], 0u}), s);
    float m = s[0];
#pragma unroll
    for (int j = 1; j < 16; j++) m = fmaxf(m, s[j]);
    mrow[a] = m + 64.0f;
    PIN(mrow[a]);
  }
  auto setup = [&]() {
#pragma unroll
    for (int a = 0; a < 4; a++) {
      auto sw = __builtin_amdgcn_permlane32_swap(__float_as_uint(mrow[a]), __float_as_uint(mrow[a]), false, false);
      float m = fmaxf(__uint_as_float(sw[0]), __uint_as_float(sw[1]));
      mrow[a] = m;
      float nm = -m;
      unsigned hi = __float_as_uint(nm) & 0xffff0000u;
      float r1 = nm - __uint_as_float(hi);
      unsigned mid = __float_as_uint(r1) & 0xffff0000u;
      float r2 = r1 - __uint_as_float(mid);
      unsigned lo = __float_as_uint(r2) & 0xffff0000u;
      q1[a] = h == 0 ? u32x2{qd[a], (hi >> 16) | mid} : u32x2{lo >> 16, 0u};
      O[a] = zero16;
    }
    sA = MFMA32(kA, q0[0], zero16);
    sA = MFMA32H(kA1, __builtin_bit_cast(s16x4, q1[0]), sA);
    pX = u32x4{0u, 0u, 0u, 0u};
    vB1 = vA0;
    SCHED0();
  };
  const char* kvl = kvs + ksp * 16 * 3584;
#define KL0(t, o) (LD ? *(const bfx8*)(kvl + ((t) * 3584 + (o)) + l16) : KV0(t, o))
#define KL1(t) (LD ? *(const s16x4*)(kvl + ((t) * 3584 + 3072) + l8) : KV1(t))
  auto pair = [&](int t, auto ldc) {
    constexpr bool LD = decltype(ldc)::value;
    kB = KL0(t + 1, 0); kB1 = KL1(t + 1); vB0 = KL0(t + 1, 1024);
    SCHED0();
    ATT_STEP(sA, sB, kA, kA1, q0[1], q1[1], O[0], vA0, O[3], vB1, pX, pY);
    vB1 = KL0(t + 1, 2048);
    SCHED0();
    ATT_STEP(sB, sA, kA, kA1, q0[2], q1[2], O[1], vA0, O[0], vA1, pY, pX);
    ATT_STEP(sA, sB, kA, kA1, q0[3], q1[3], O[2], vA0, O[1], vA1, pX, pY);
    ATT_STEP(sB, sA, kB, kB1, q0[0], q1[0], O[3], vA0, O[2], vA1, pY, pX);
    const int t2 = t < 14 ? t + 2 : 15;
    kA = KL0(t2, 0); kA1 = KL1(t2); vA0 = KL0(t2, 1024);
    SCHED0();
    ATT_STEP(sA, sB, kB, kB1, q0[1], q1[1], O[0], vB0, O[3], vA1, pX, pY);
    vA1 = KL0(t2, 2048);
    SCHED0();
    ATT_STEP(sB, sA, kB, kB1, q0[2], q1[2], O[1], vB0, O[0], vB1, pY, pX);
    ATT_STEP(sA, sB, kB, kB1, q0[3], q1[3], O[2], vB0, O[1], vB1, pX, pY);
    ATT_STEP(sB, sA, kA, kA1, q0[0], q1[0], O[3], vB0, O[2], vB1, pY, pX);
  };
  for (int attempt = 0; attempt < 2; attempt++) {
    if (w < 4) { POLL4(cnt[1]); vA0 = *(const bfx8*)(kvl + 1024 + l16); vA1 = *(const bfx8*)(kvl + 2048 + l16); SCHED0(); }
    else { vA0 = KV0(0, 1024); vA1 = KV0(0, 2048); }
    setup();
    STAMP(1, 2);
    if (w < 4) { for (int t = 0; t < 16; t += 2) { if (t == 2) POLL4(cnt[2]); pair(t, BoolC<true>{}); } }
    else { __builtin_amdgcn_s_setprio(BPRIO); for (int t = 0; t < 16; t += 2) pair(t, BoolC<false>{}); }
    O[3] = MFMA32(vB1, __builtin_bit_cast(bfx8, pX), O[3]);
    float chk = 0.f, lmax = 0.f;
#pragma unroll
    for (int a = 0; a < 4; a++) {
#pragma unroll
      for (int j = 0; j < 11; j++) chk += fabsf(O[a][j]);
      lmax = fmaxf(lmax, h == 0 ? O[a][10] : O[a][8]);
    }
    bool bad = !(lmax < 1.0e33f) || !(chk < 3.0e38f);
    if (attempt == 1 || !__any(bad)) break;
#pragma unroll
    for (int a = 0; a < 4; a++) mrow[a] = -INFINITY;
    for (int t = 0; t < 16; t++) {
      bfx8 k0 = KV0(t, 0);
      s16x4 k1 = KV1(t);
#pragma unroll
      for (int a = 0; a < 4; a++) {
        f32x16 s = MFMA32(k0, q0[a], zero16);
        s = MFMA32H(k1, __builtin_bit_cast(s16x4, u32x2{qd[a], 0u}), s);
#pragma unroll
        for (int j = 0; j < 16; j++) mrow[a] = fmaxf(mrow[a], s[j]);
      }
    }
    kA = KV0(0, 0); kA1 = KV1(0);
  }
  STAMP(1, 3);
  __syncthreads();
#pragma unroll
  for (int a = 0; a < 4; a++) {
    float* p = part + ((ksp * 8 + qh * 4 + a) * 32 + r) * 20;
    *(f32x4*)(p + 4 * h) = f32x4{O[a][0], O[a][1], O[a][2], O[a][3]};
    *(f32x4*)(p + 8 + 4 * h) = f32x4{O[a][4], O[a][5], O[a][6], O[a][7]};
    if (h == 0) *(f32x4*)(p + 16) = f32x4{O[a][8], O[a][9], O[a][10], mrow[a]};
  }
  const float gt = ((const float*)(ws + WS_G))[b * 8 + w];
  __syncthreads();
  {
    const int row = w * 32 + r;
    const float* p = part + row * 20;
    f32x4 tl[4]; float L[4], M = -INFINITY;
#pragma unroll
    for (int k = 0; k < 4; k++) {
      tl[k] = *(const f32x4*)(p + k * 5120 + 16);
      L[k] = tl[k][3] + __builtin_amdgcn_logf(tl[k][2]);
      M = fmaxf(M, L[k]);
    }
    float den = 0.f;
#pragma unroll
    for (int k = 0; k < 4; k++) { L[k] = EX2(L[k] - M); den += L[k]; }
    const float inv = gt * __builtin_amdgcn_rcpf(den);
    f32x4 o0 = {0.f, 0.f, 0.f, 0.f}, o1 = o0; float o2 = 0.f, o3 = 0.f;
#pragma unroll
    for (int k = 0; k < 4; k++) {
      float sc = L[k] * inv * __builtin_amdgcn_rcpf(tl[k][2]);
      o0 += *(const f32x4*)(p + k * 5120 + 8 * h) * sc;
      o1 += *(const f32x4*)(p + k * 5120 + 8 * h + 4) * sc;
      o2 += tl[k][0] * sc; o3 += tl[k][1] * sc;
    }
    unsigned* ob = obuf + w * 288 + r * 9 + 4 * h;
    ob[0] = pk2(o0[0], o0[1]); ob[1] = pk2(o0[2], o0[3]); ob[2] = pk2(o1[0], o1[1]); ob[3] = pk2(o1[2], o1[3]);
    if (h == 0) ob[8] = pk2(o2, o3);
  }
  __syncthreads();
  for (int c = tid; c < 576; c += 512) {
    int tl = c & 7, ch = c >> 3, t = b * 8 + tl;
    char* dst = ws + WS_AH + ((size_t)(t >> 4) * 90 + (ch >> 2)) * 1024 + ((t & 15) + 16 * (ch & 3)) * 16;
    *(u32x4*)dst = *(const u32x4*)(obuf + tl * 288 + ch * 4);
  }
  STAMPE(1, 4);
}

constexpr int K3_ST = 34816, K3_NS = 4;

__global__ __launch_bounds__(512, 2) void k3_spatial(const u16* __restrict__ x, const u16* __restrict__ w_sp,
                                                     const u16* __restrict__ b_sp, char* __restrict__ ws) {
  __shared__ __attribute__((aligned(1024))) char sm[K3_ST * K3_NS];
  const int tid = threadIdx.x, lane = tid & 63;
  const int w = __builtin_amdgcn_readfirstlane(tid >> 6);
  const int b = blockIdx.x, xcd = b & 7, ii = b >> 3;
  const int rt = (xcd & 1) * 8 + (ii & 7), ct = (xcd >> 1) * 4 + (ii >> 3);
  if (w >= 4) {
    const int lw = w - 4, r8 = lane >> 3, p = lane & 7;
    const int nb = lw < 2 ? 5 : 4, b0 = lw < 2 ? lw * 5 : 10 + (lw - 2) * 4;
    const char* a1 = (const char*)x + (size_t)(rt * 128 + lw * 32 + r8) * 1152 + (p ^ r8) * 16;
    const char* a2 = (const char*)w_sp + (size_t)(rt * 128 + lw * 32 + r8) * 4096 + (p ^ r8) * 16;
    const char* b1 = ws + WS_W1 + (size_t)ct * 9 * 18 * 1024 + lane * 16;
    const char* b2 = ws + WS_Z2 + (size_t)ct * 9 * 64 * 1024 + lane * 16;
#define KOFF (5 * (rt & 7))
    const int koff = KOFF;
    auto issue = [&](int j) {
      char* st = sm + (j & 3) * K3_ST;
      if (j < 9) {
#pragma unroll
        for (int u = 0; u < 4; u++) glds16(a1 + u * 8 * 1152 + j * 128, st + (lw * 4 + u) * 1024);
#pragma unroll
        for (int u = 0; u < 5; u++)
          if (u < nb) { int pc = b0 + u; glds16(b1 + ((pc >> 1) * 18 + 2 * j + (pc & 1)) * 1024, st + 16384 + pc * 1024); }
      } else {
        const int kk = (j - 9 + koff) & 31;
#pragma unroll
        for (int u = 0; u < 4; u++) glds16(a2 + u * 8 * 4096 + kk * 128, st + (lw * 4 + u) * 1024);
#pragma unroll
        for (int u = 0; u < 5; u++)
          if (u < nb) { int pc = b0 + u; glds16(b2 + ((pc >> 1) * 64 + 2 * kk + (pc & 1)) * 1024, st + 16384 + pc * 1024); }
      }
    };
    issue(0); issue(1); issue(2); issue(3);
    if (lw < 2) WAITV(27); else WAITV(24);
    BAR();
    for (int j = 0; j < 38; j++) {
      if (lw < 2) WAITV(18); else WAITV(16);
      BAR();
      if (j + 4 < 41) issue(j + 4);
    }
    if (lw < 2) WAITV(9); else WAITV(8);
    BAR();
    WAITV(0); BAR();
    BAR();
    return;
  }
  const int i = lane & 15, g = lane >> 4;
  f32x4 acc[9][2];
  unsigned z1p[9][2][2];
#pragma unroll
  for (int n = 0; n < 9; n++) acc[n][0] = acc[n][1] = f32x4{0.f, 0.f, 0.f, 0.f};
  const int aoff0 = (w * 32 + i) * 128 + ((g ^ (i & 7)) * 16), aoff1 = aoff0 ^ 64;
  const int boff = 16384 + lane * 16;
  bfx8 Af[2][2], Bf[2][9];
  auto rd = [&](int buf, char* st, int ks) {
#pragma unroll
    for (int m = 0; m < 2; m++) Af[buf][m] = *(const bfx8*)(st + (ks ? aoff1 : aoff0) + m * 2048);
#pragma unroll
    for (int n = 0; n < 9; n++) Bf[buf][n] = *(const bfx8*)(st + boff + (n * 2 + ks) * 1024);
  };
  auto mm = [&](int buf) {
#pragma unroll
    for (int n = 0; n < 9; n++)
#pragma unroll
      for (int m = 0; m < 2; m++)
        acc[n][m] = MFMA16(Bf[buf][n], Af[buf][m], acc[n][m]);
  };
  auto ilv = [&]() {
#pragma unroll
    for (int x2 = 0; x2 < 11; x2++) {
      __builtin_amdgcn_sched_group_barrier(0x8, 1, 0);
      __builtin_amdgcn_sched_group_barrier(0x100, 1, 0);
    }
    __builtin_amdgcn_sched_group_barrier(0x8, 7, 0);
  };
  auto body = [&](int j) {
    char* st = sm + (j & 3) * K3_ST;
    rd(1, st, 1); mm(0); ilv();
    SCHED0(); WAITL0(); BAR(); SCHED0();
    rd(0, sm + ((j + 1) & 3) * K3_ST, 0);
    mm(1); ilv();
    SCHED0();
  };
  STAMP0(); STAMP(2, 0);
  BAR(); SCHED0();
  STAMP(2, 1);
  rd(0, sm, 0);
  SCHED0();
  for (int j = 0; j < 9; j++) body(j);
  STAMP(2, 2);
#pragma unroll
  for (int n = 0; n < 9; n++)
#pragma unroll
    for (int m = 0; m < 2; m++) {
      f32x4 a = acc[n][m];
      z1p[n][m][0] = pk2(gelu_t(a[0]), gelu_t(a[1]));
      z1p[n][m][1] = pk2(gelu_t(a[2]), gelu_t(a[3]));
      acc[n][m] = f32x4{0.f, 0.f, 0.f, 0.f};
    }
  SCHED0();
  for (int j = 9; j < 41; j++) body(j);
  STAMP(2, 3);
#pragma unroll
  for (int m = 0; m < 2; m++) {
    const int t = rt * 128 + w * 32 + m * 16 + i;
    const float og = 1.0f - ((const float*)(ws + WS_G))[t];
    const float bs = __uint_as_float((unsigned)b_sp[t] << 16);
    char* dst = ws + WS_AH + (size_t)(t >> 4) * 90 * 1024 + i * 16 + (g & 1) * 8;
#pragma unroll
    for (int n = 0; n < 9; n++) {
      f32x4 a = acc[n][m];
      int f0 = ct * 144 + n * 16;
      u32x2 o = {pk2(og * bflo(z1p[n][m][0]) * (a[0] + bs), og * bfhi(z1p[n][m][0]) * (a[1] + bs)),
                 pk2(og * bflo(z1p[n][m][1]) * (a[2] + bs), og * bfhi(z1p[n][m][1]) * (a[3] + bs))};
      *(u32x2*)(dst + (18 + (f0 >> 5)) * 1024 + (((f0 & 31) >> 3) + (g >> 1)) * 256) = o;
    }
  }
  STAMPE(2, 4);
}

template <int NT>
__device__ __forceinline__ void k4_body(float* red, const char* ws, u16* out, int rt, int nt0, int w, int lane) {
  const int kt0 = w < 2 ? w * 23 : 46 + (w - 2) * 22, nk = w < 2 ? 23 : 22;
  const char* ap = ws + WS_AH + ((size_t)rt * 4 * 90 + kt0) * 1024 + lane * 16;
  const char* bp = ws + WS_WO + ((size_t)nt0 * 90 + kt0) * 1024 + lane * 16;
  f32x4 acc[4][NT];
#pragma unroll
  for (int m = 0; m < 4; m++)
#pragma unroll
    for (int n = 0; n < NT; n++) acc[m][n] = f32x4{0.f, 0.f, 0.f, 0.f};
  bfx8 Af[3][4], Bf[3][NT];
  auto ld = [&](int buf, int k) {
    int kk = k < nk ? k : nk - 1;
#pragma unroll
    for (int m = 0; m < 4; m++) Af[buf][m] = *(const bfx8*)(ap + (size_t)(m * 90 + kk) * 1024);
#pragma unroll
    for (int n = 0; n < NT; n++) Bf[buf][n] = *(const bfx8*)(bp + (size_t)(n * 90 + kk) * 1024);
  };
  auto mm = [&](int buf) {
#pragma unroll
    for (int m = 0; m < 4; m++)
#pragma unroll
      for (int n = 0; n < NT; n++) acc[m][n] = MFMA16(Bf[buf][n], Af[buf][m], acc[m][n]);
  };
  STAMP0(); STAMP(3, 0);
  ld(0, 0); ld(1, 1); ld(2, 2);
  for (int k = 0; k < 21; k += 3) {
    SCHED0(); mm(0); SCHED0(); ld(0, k + 3);
    SCHED0(); mm(1); SCHED0(); ld(1, k + 4);
    SCHED0(); mm(2); SCHED0(); if (k < 18) ld(2, k + 5);
  }
  SCHED0();
  mm(0);
  if (nk == 23) mm(1);
  STAMP(3, 1);
  constexpr int LD = NT * 16 + 4;
  const int i = lane & 15, g = lane >> 4;
#pragma unroll
  for (int m = 0; m < 4; m++)
#pragma unroll
    for (int n = 0; n < NT; n++) *(f32x4*)(red + (size_t)(w * 64 + m * 16 + i) * LD + n * 16 + g * 4) = acc[m][n];
  __syncthreads();
  const int tid = w * 64 + lane;
  for (int c = tid; c < 64 * NT * 2; c += 256) {
    int row = c / (NT * 2), ch = c % (NT * 2);
    const float* p = red + row * LD + ch * 8;
    f32x4 s0 = *(const f32x4*)p, s1 = *(const f32x4*)(p + 4);
#pragma unroll
    for (int ww = 1; ww < 4; ww++) { s0 += *(const f32x4*)(p + ww * 64 * LD); s1 += *(const f32x4*)(p + ww * 64 * LD + 4); }
    *(u32x4*)(out + (size_t)(rt * 64 + row) * 576 + nt0 * 16 + ch * 8) =
        u32x4{pk2(s0[0], s0[1]), pk2(s0[2], s0[3]), pk2(s1[0], s1[1]), pk2(s1[2], s1[3])};
  }
  STAMPE(3, 2);
}

__global__ __launch_bounds__(256, 1) void k4_out(const char* __restrict__ ws, u16* __restrict__ out) {
  __shared__ __attribute__((aligned(16))) float red[4 * 64 * 84];
  const int lane = threadIdx.x & 63;
  const int w = __builtin_amdgcn_readfirstlane(threadIdx.x >> 6);
  const int b = blockIdx.x, xcd = b & 7, ii = b >> 3;
  const int rt = xcd * 4 + (ii & 3), cg = ii >> 2;
  const int nt0 = (cg >> 1) * 9 + (cg & 1) * 5;
  if (cg & 1) k4_body<4>(red, ws, out, rt, nt0, w, lane);
  else k4_body<5>(red, ws, out, rt, nt0, w, lane);
}

extern "C" void kernel_launch(void* const* d_in, const int* in_sizes, int n_in, void* d_out, int out_size,
                              void* d_ws, size_t ws_size, hipStream_t stream) {
  const u16* x = (const u16*)d_in[0];
  const u16* wq = (const u16*)d_in[1];
  const u16* wk = (const u16*)d_in[2];
  const u16* wv = (const u16*)d_in[3];
  const u16* wo = (const u16*)d_in[4];
  const u16* w_in = (const u16*)d_in[6];
  const u16* w_sp = (const u16*)d_in[7];
  const u16* b_sp = (const u16*)d_in[8];
  const u16* w_out = (const u16*)d_in[9];
  const u16* gw = (const u16*)d_in[10];
  char* ws = (char*)d_ws;
  k1_proj<<<256, 512, 0, stream>>>(x, wq, wk, wv, w_in, gw, ws);
  k2_attn<<<256, 512, 0, stream>>>(w_in, wo, w_out, w_sp, ws);
  k3_spatial<<<256, 512, 0, stream>>>(x, w_sp, b_sp, ws);
  k4_out<<<256, 256, 0, stream>>>(ws, (u16*)d_out);
}
```

```cpp
#include <hip/hip_runtime.h>
#include <stdint.h>

typedef __attribute__((ext_vector_type(8))) __bf16 bfx8;
typedef __attribute__((ext_vector_type(2))) __bf16 bfx2;
typedef __attribute__((ext_vector_type(4))) short s16x4;
typedef __attribute__((ext_vector_type(4))) float f32x4;
typedef __attribute__((ext_vector_type(16))) float f32x16;
typedef __attribute__((ext_vector_type(4))) unsigned u32x4;
typedef __attribute__((ext_vector_type(2))) unsigned u32x2;
typedef u32x4 __attribute__((aligned(4))) u32x4_a4;
typedef unsigned short u16;
#define LDSP __attribute__((address_space(3)))
#define GLBP __attribute__((address_space(1)))

constexpr int S_ = 2048, D_ = 576, FH = 2304;
constexpr size_t WS_Q   = 0;
constexpr size_t WS_KV  = WS_Q + (size_t)S_ * D_ * 2;
constexpr size_t WS_G   = WS_KV + 64 * 3584;
constexpr size_t WS_Z2  = WS_G + 2048 * 4;
constexpr size_t WS_W1  = WS_Z2 + (size_t)144 * 64 * 1024;
constexpr size_t WS_WO  = WS_W1 + (size_t)144 * 18 * 1024;
constexpr size_t WS_AH  = WS_WO + (size_t)36 * 90 * 1024;
constexpr size_t WS_ZN  = WS_AH + (size_t)128 * 90 * 1024;
constexpr size_t WS_CN  = WS_ZN + (size_t)144 * 64 * 1024;
constexpr size_t WS_END = WS_CN + 64 * 128;
typedef GLBP unsigned gu32;

__device__ __forceinline__ unsigned pk2(float a, float b) { bfx2 r; r[0] = (__bf16)a; r[1] = (__bf16)b; return __builtin_bit_cast(unsigned, r); }
__device__ __forceinline__ float bflo(unsigned u) { return __uint_as_float(u << 16); }
__device__ __forceinline__ float bfhi(unsigned u) { return __uint_as_float(u & 0xffff0000u); }
__device__ __forceinline__ float gelu_t(float x) {
  const float c0 = -2.302208198f;
  const float c1 = c0 * 0.044715f;
  float e = __builtin_amdgcn_exp2f(x * __builtin_fmaf(x * x, c1, c0));
  return x * __builtin_amdgcn_rcpf(1.0f + e);
}
__device__ __forceinline__ void glds16(const void* g, char* l) {
  __builtin_amdgcn_global_load_lds((const GLBP void*)g, (LDSP void*)l, 16, 0, 0);
}
#define ST16(v, off) __builtin_amdgcn_raw_buffer_store_b128(v, rs_, (unsigned)(off), 0, 16)
#define ST8(v, off) __builtin_amdgcn_raw_buffer_store_b64(v, rs_, (unsigned)(off), 0, 16)
#define LD16(off, so) __builtin_amdgcn_raw_buffer_load_b128(rs_, (unsigned)(off), so, 16)
#define SIGNAL(c) __hip_atomic_fetch_add((gu32*)(ws + WS_CN + (c) * 128), 1u, __ATOMIC_RELAXED, __HIP_MEMORY_SCOPE_AGENT)
#define AWAIT(c, n) do { while (__hip_atomic_load((gu32*)(ws + WS_CN + (c) * 128), __ATOMIC_RELAXED, __HIP_MEMORY_SCOPE_AGENT) < (n)) \
  __builtin_amdgcn_s_sleep(4); asm volatile("" ::: "memory"); } while (0)
#define MFMA16(a, b, c) __builtin_amdgcn_mfma_f32_16x16x32_bf16(a, b, c, 0, 0, 0)
#define MFMA32(a, b, c) __builtin_amdgcn_mfma_f32_32x32x16_bf16(a, b, c, 0, 0, 0)
#define MFMA32H(a, b, c) __builtin_amdgcn_mfma_f32_32x32x8bf16_1k(a, b, c, 0, 0, 0)
#define WAITV(n) asm volatile("s_waitcnt vmcnt(" #n ")" ::: "memory")
#define WAITL0() asm volatile("s_waitcnt lgkmcnt(0)" ::: "memory")
#define BAR() __builtin_amdgcn_s_barrier()
#define SCHED0() __builtin_amdgcn_sched_barrier(0)
#define STPARM
#define STARG
#define STAMP0()
#define STAMP(k, i)
#define STAMPE(k, n)

constexpr int K1_ST = 40960, K1_NS = 3;

template <bool ISQ>
__device__ __forceinline__ void k1_consumer(char* sm, int w, int lane, int rt, int ct, char* ws) {
  const int wm = w & 1, wn = w >> 1, i = lane & 15, g = lane >> 4;
  f32x4 acc[4][6];
#pragma unroll
  for (int m = 0; m < 4; m++)
#pragma unroll
    for (int n = 0; n < 6; n++) acc[m][n] = f32x4{0.f, 0.f, 0.f, 0.f};
  const int arow = wm * 64 + 8 * (i >> 2) + (i & 3);
  const int aoff0 = arow * 128 + ((g ^ (i & 7)) * 16), aoff1 = aoff0 ^ 64;
  int boff[6];
  {
    const int q = i >> 2, p = i & 3, h = ((q >> 1) & 1) | ((g & 1) << 1);
#pragma unroll
    for (int n = 0; n < 6; n++) {
      int nt = wn * 6 + n;
      boff[n] = 16384 + (nt >> 2) * 8192 + g * 1024 + q * 128 + (((nt & 3) ^ h) * 32) + p * 8;
    }
  }
  bfx8 Af[2][4], Bf[2][6];
  auto rd = [&](int buf, char* st, int ks) {
#pragma unroll
    for (int n = 0; n < 6; n++) {
      s16x4 lo = __builtin_amdgcn_ds_read_tr16_b64_v4i16((LDSP s16x4*)(st + boff[n] + ks * 4096));
      s16x4 hi = __builtin_amdgcn_ds_read_tr16_b64_v4i16((LDSP s16x4*)(st + boff[n] + ks * 4096 + 512));
      Bf[buf][n] = __builtin_bit_cast(bfx8, __builtin_shufflevector(lo, hi, 0, 1, 2, 3, 4, 5, 6, 7));
    }
#pragma unroll
    for (int m = 0; m < 4; m++)
      Af[buf][m] = *(const bfx8*)(st + (ks ? aoff1 : aoff0) + (4 * (m & 1) + 32 * (m >> 1)) * 128);
  };
  auto mm = [&](int buf) {
#pragma unroll
    for (int m = 0; m < 4; m++)
#pragma unroll
      for (int n = 0; n < 6; n++)
        acc[m][n] = ISQ ? MFMA16(Bf[buf][n], Af[buf][m], acc[m][n]) : MFMA16(Af[buf][m], Bf[buf][n], acc[m][n]);
  };
  auto ilv = [&]() {
#pragma unroll
    for (int x = 0; x < 8; x++) {
      __builtin_amdgcn_sched_group_barrier(0x8, 2, 0);
      __builtin_amdgcn_sched_group_barrier(0x100, 2, 0);
    }
    __builtin_amdgcn_sched_group_barrier(0x8, 8, 0);
  };
  STAMP0(); STAMP(0, 0);
  BAR(); SCHED0();
  STAMP(0, 1);
  rd(0, sm, 0);
  SCHED0();
#pragma unroll
  for (int kt = 0; kt < 9; kt++) {
    char* st = sm + (kt % K1_NS) * K1_ST;
    rd(1, st, 1); mm(0); ilv();
    SCHED0(); WAITL0(); BAR(); SCHED0();
    if (kt < 8) rd(0, sm + ((kt + 1) % K1_NS) * K1_ST, 0);
    mm(1);
    if (kt < 8) ilv();
    SCHED0();
  }
  STAMP(0, 2);
  if (!ISQ) {
    char* z2 = ws + WS_Z2;
#pragma unroll
    for (int n = 0; n < 6; n++)
#pragma unroll
      for (int mp = 0; mp < 2; mp++) {
        f32x4 a = acc[2 * mp][n], b = acc[2 * mp + 1][n];
        u32x4 o = {pk2(gelu_t(a[0]), gelu_t(a[1])), pk2(gelu_t(a[2]), gelu_t(a[3])),
                   pk2(gelu_t(b[0]), gelu_t(b[1])), pk2(gelu_t(b[2]), gelu_t(b[3]))};
        int ft = ct * 12 + wn * 6 + n, kt32 = rt * 4 + wm * 2 + mp;
        *(u32x4*)(z2 + ((size_t)(ft * 64 + kt32) * 64 + lane) * 16) = o;
      }
  } else {
    const float c = 0.34004649f;
    char* q = ws + WS_Q;
#pragma unroll
    for (int m = 0; m < 4; m++) {
      int t = rt * 128 + arow + 4 * (m & 1) + 32 * (m >> 1);
#pragma unroll
      for (int n = 0; n < 6; n++) {
        f32x4 a = acc[m][n];
        u32x2 o = {pk2(a[0] * c, a[1] * c), pk2(a[2] * c, a[3] * c)};
        *(u32x2*)(q + ((size_t)t * 576 + (ct - 12) * 192 + wn * 96 + n * 16 + g * 4) * 2) = o;
      }
    }
  }
  STAMPE(0, 3);
}

__global__ __launch_bounds__(512, 2) void k1_proj(const u16* __restrict__ x, const u16* __restrict__ wq,
                                                  const u16* __restrict__ wk, const u16* __restrict__ wv,
                                                  const u16* __restrict__ w_in, const u16* __restrict__ gw,
                                                  char* __restrict__ ws) {
  __shared__ __attribute__((aligned(1024))) char sm[K1_ST * K1_NS];
  const int tid = threadIdx.x, lane = tid & 63;
  const int w = __builtin_amdgcn_readfirstlane(tid >> 6);
  const int b = blockIdx.x, xcd = b & 7, ii = b >> 3;
  const int rt = (xcd & 1) * 8 + (ii & 7), ct = (xcd >> 1) * 4 + (ii >> 3);
  if (b == 0 && tid < 64) *(unsigned*)(ws + WS_CN + tid * 128) = 0u;
  if (ct == 15) {
    const int i = lane & 15, g = lane >> 4;
    bfx8 Af[18];
    const u16* xr = x + (size_t)(rt * 128 + w * 16 + i) * 576 + g * 8;
#pragma unroll
    for (int k = 0; k < 18; k++) Af[k] = *(const bfx8*)(xr + k * 32);
    for (int fr = w; fr < 54; fr += 8) {
      int nt = fr / 18, kt = fr % 18, n = nt * 16 + i, k0 = kt * 32 + g * 8;
      const u16* src = n < 18 ? wk + n : (n < 36 ? wv + (n - 18) : gw);
      int str = n < 36 ? 18 : 1;
      unsigned v[8];
#pragma unroll
      for (int j = 0; j < 8; j++) v[j] = src[(k0 + j) * str];
      u32x4 o = {v[0] | (v[1] << 16), v[2] | (v[3] << 16), v[4] | (v[5] << 16), v[6] | (v[7] << 16)};
      if (n > 36) o = u32x4{0, 0, 0, 0};
      *(u32x4*)(sm + fr * 1024 + lane * 16) = o;
    }
    __syncthreads();
    f32x4 acc[3] = {{0.f, 0.f, 0.f, 0.f}, {0.f, 0.f, 0.f, 0.f}, {0.f, 0.f, 0.f, 0.f}};
#pragma unroll
    for (int k = 0; k < 18; k++)
#pragma unroll
      for (int n = 0; n < 3; n++) acc[n] = MFMA16(Af[k], *(const bfx8*)(sm + (n * 18 + k) * 1024 + lane * 16), acc[n]);
    float* res = (float*)(sm + 56 * 1024);
#pragma unroll
    for (int n = 0; n < 3; n++)
#pragma unroll
      for (int r = 0; r < 4; r++) res[(w * 16 + g * 4 + r) * 49 + n * 16 + i] = acc[n][r];
    __syncthreads();
    char* kv = ws + WS_KV + (size_t)rt * 4 * 3584;
    if (tid < 128) {
      float s = res[tid * 49 + 36];
      ((float*)(ws + WS_G))[rt * 128 + tid] = __builtin_amdgcn_rcpf(1.0f + __builtin_amdgcn_exp2f(-1.44269504f * s));
    }
    {
      const int tile = w >> 1, r = lane & 31, h = lane >> 5;
      const float* rb = res + tile * 32 * 49;
      char* dst = kv + tile * 3584;
      if ((w & 1) == 0) {
        const float* p = rb + r * 49 + 8 * h;
        u32x4 o = {pk2(p[0], p[1]), pk2(p[2], p[3]), pk2(p[4], p[5]), pk2(p[6], p[7])};
        *(u32x4*)(dst + lane * 16) = o;
        u32x2 o2;
        if (h == 0) o2 = u32x2{pk2(rb[r * 49 + 16], rb[r * 49 + 17]), 0x3f803f80u};
        else o2 = u32x2{0x00003f80u, 0u};
        *(u32x2*)(dst + 3072 + lane * 8) = o2;
      } else {
#pragma unroll
        for (int s = 0; s < 2; s++) {
          float v[8];
#pragma unroll
          for (int j = 0; j < 8; j++) {
            int key = 16 * s + 8 * (j >> 2) + 4 * h + (j & 3);
            float t = rb[key * 49 + 18 + (r < 18 ? r : 0)];
            v[j] = r < 18 ? t : ((r == 18 || r == 20) ? 1.0f : 0.0f);
          }
          u32x4 o = {pk2(v[0], v[1]), pk2(v[2], v[3]), pk2(v[4], v[5]), pk2(v[6], v[7])};
          *(u32x4*)(dst + 1024 + s * 1024 + lane * 16) = o;
        }
      }
    }
    return;
  }
  if (w >= 4) {
    const int lw = w - 4;
    const u16* W = ct < 12 ? w_in + FH + ct * 192 : wq + (ct - 12) * 192;
    const int ldw = ct < 12 ? 4608 : 576;
    const int r8 = lane >> 3, p = lane & 7;
    const u16* asrc[4];
    const u16* bsrc[6];
#pragma unroll
    for (int j = 0; j < 4; j++) {
      int row = (lw * 4 + j) * 8 + r8;
      int f = (row & 3) | (((row >> 3) & 1) << 2);
      asrc[j] = x + (size_t)(rt * 128 + row) * 576 + (p ^ f) * 8;
    }
#pragma unroll
    for (int j = 0; j < 6; j++) {
      int st = lw * 6 + j, ng = st >> 3, kb = st & 7;
      int h = ((r8 >> 1) & 1) | ((kb & 1) << 1);
      bsrc[j] = W + (size_t)(kb * 8 + r8) * ldw + ng * 64 + (p ^ (h << 1)) * 8;
    }
    auto issue = [&](int kt) {
      char* st = sm + (kt % K1_NS) * K1_ST;
#pragma unroll
      for (int j = 0; j < 4; j++) glds16(asrc[j] + kt * 64, st + (lw * 4 + j) * 1024);
#pragma unroll
      for (int j = 0; j < 6; j++) glds16(bsrc[j] + (size_t)kt * 64 * ldw, st + 16384 + (lw * 6 + j) * 1024);
    };
    issue(0); issue(1); issue(2);
    WAITV(20); BAR();
#pragma unroll
    for (int kt = 0; kt < 9; kt++) {
      if (kt < 7) WAITV(10); else WAITV(0);
      BAR();
      if (kt + 3 < 9) issue(kt + 3);
    }
    return;
  }
  if (ct < 12) k1_consumer<false>(sm, w, lane, rt, ct, ws);
  else k1_consumer<true>(sm, w, lane, rt, ct, ws);
}

#define ACQW 4
#define BPRIO 1
template <bool B> struct BoolC { static constexpr bool value = B; };
template <int CTRL>
__device__ __forceinline__ float dpp_add(float v) {
  return v + __builtin_bit_cast(float, __builtin_amdgcn_update_dpp(0, __builtin_bit_cast(int, v), CTRL, 0xf, 0xf, true));
}
#define POLL4(c) do { while (__hip_atomic_load(&(c), __ATOMIC_RELAXED, __HIP_MEMORY_SCOPE_WORKGROUP) < 4u) __builtin_amdgcn_s_sleep(1); asm volatile("" ::: "memory"); } while (0)
#define PIN(x) asm volatile("" : "+v"(x))
#define EX2(x) __builtin_amdgcn_exp2f(x)
#define ATT_STEP(sc, sn, K0, K1, Q0, Q1, Ocur, V0, Oprev, V1p, pprev, pcur)                          \
  {                                                                                                  \
    u32x4 pa;                                                                                        \
    sn = MFMA32(K0, Q0, zero16); PIN(sn); SCHED0();                                                  \
    { float e0 = EX2(sc[0]), e1 = EX2(sc[1]), e2 = EX2(sc[2]), e3 = EX2(sc[3]);                      \
      pa[0] = pk2(e0, e1); pa[1] = pk2(e2, e3); } PIN(pa); SCHED0();                                  \
    sn = MFMA32H(K1, __builtin_bit_cast(s16x4, Q1), sn); PIN(sn); SCHED0();                          \
    { float e0 = EX2(sc[4]), e1 = EX2(sc[5]), e2 = EX2(sc[6]), e3 = EX2(sc[7]);                      \
      pa[2] = pk2(e0, e1); pa[3] = pk2(e2, e3); } PIN(pa); SCHED0();                                  \
    Oprev = MFMA32(V1p, __builtin_bit_cast(bfx8, pprev), Oprev); PIN(Oprev); SCHED0();               \
    { float e0 = EX2(sc[8]), e1 = EX2(sc[9]), e2 = EX2(sc[10]), e3 = EX2(sc[11]);                    \
      pcur[0] = pk2(e0, e1); pcur[1] = pk2(e2, e3); } PIN(pcur); SCHED0();                           \
    Ocur = MFMA32(V0, __builtin_bit_cast(bfx8, pa), Ocur); PIN(Ocur); SCHED0();                      \
    { float e0 = EX2(sc[12]), e1 = EX2(sc[13]), e2 = EX2(sc[14]), e3 = EX2(sc[15]);                  \
      pcur[2] = pk2(e0, e1); pcur[3] = pk2(e2, e3); } PIN(pcur); SCHED0();                           \
  }

constexpr int K3_ST = 34816;
#define ALD16(d, vo, so, fl) asm volatile("buffer_load_dwordx4 %0, %1, %2, %3 offen" fl : "=v"(d) : "v"(vo), "s"(rsv), "s"(so) : "memory")

__device__ __forceinline__ void k4_body(char* sm, char* ws, u32x4 rsv, u16* out, int rt, int nt0, int nl, int w, int lane,
                                        f32x4 (&acc)[4][5], bfx8 (&Bf)[3][5] STPARM) {
  constexpr int NT = 5;
  bfx8 Af[5][4], Bl[NT];
  const int l16 = lane * 16, k0 = 18 + w * 9;
  auto lda = [&](int buf, int k) {
#pragma unroll
    for (int m = 0; m < 4; m++) ALD16(Af[buf][m], l16, (int)WS_AH + ((rt * 4 + m) * 90 + k0 + k) * 1024, " sc1");
  };
  auto ldb = [&](int buf, int k) {
#pragma unroll
    for (int n = 0; n < NT; n++) ALD16(Bf[buf][n], l16, (int)WS_WO + ((nt0 + (n < 4 ? n : nl)) * 90 + k0 + k) * 1024, "");
  };
  auto mm = [&](bfx8 (&B)[NT], int buf) {
#pragma unroll
    for (int m = 0; m < 4; m++) PIN(Af[buf][m]);
#pragma unroll
    for (int n = 0; n < NT; n++) PIN(B[n]);
#pragma unroll
    for (int m = 0; m < 4; m++)
#pragma unroll
      for (int n = 0; n < NT; n++) acc[m][n] = MFMA16(B[n], Af[buf][m], acc[m][n]);
    SCHED0();
  };
  auto rdl = [&](int k) {
#pragma unroll
    for (int n = 0; n < NT; n++) Bl[n] = *(const bfx8*)(sm + K3_ST + ((w * 3 + k) * 5 + n) * 1024 + l16);
  };
  STAMP(1, 1);
  if (w == 4) AWAIT(33 + (rt >> 1), 16u);
  BAR(); asm volatile("" ::: "memory");
  STAMP(1, 2);
  rdl(0); SCHED0();
  lda(0, 0); lda(1, 1); lda(2, 2); lda(3, 3); lda(4, 4);
  WAITV(16); mm(Bf[0], 0); lda(0, 5); ldb(0, 6);
  WAITV(21); mm(Bf[1], 1); lda(1, 6); ldb(1, 7);
  WAITV(26); mm(Bf[2], 2); lda(2, 7); ldb(2, 8);
  WAITV(31); mm(Bl, 3); lda(3, 8); rdl(1);
  WAITV(31); mm(Bl, 4); rdl(2);
  WAITV(27); mm(Bl, 0);
  BAR();
  WAITV(18); mm(Bf[0], 1);
  WAITV(9); mm(Bf[1], 2);
  WAITV(0); mm(Bf[2], 3);
  STAMP(1, 3);
#pragma unroll
  for (int m = 0; m < 4; m++)
#pragma unroll
    for (int n = 0; n < NT; n++) *(f32x4*)(sm + (w * 4 * NT + m * NT + n) * 1024 + lane * 16) = acc[m][n];
  WAITL0(); BAR();
  for (int c = w * 64 + lane; c < 128 * NT; c += 512) {
    const int fr = c >> 5, i = c & 15, gp = (c >> 4) & 1;
    const char* p = sm + fr * 1024 + (i + 32 * gp) * 16;
    f32x4 s0 = *(const f32x4*)p, s1 = *(const f32x4*)(p + 256);
#pragma unroll
    for (int ww = 1; ww < 8; ww++) { s0 += *(const f32x4*)(p + ww * 4 * NT * 1024); s1 += *(const f32x4*)(p + ww * 4 * NT * 1024 + 256); }
    const int m = fr / NT, n = fr % NT;
    if (n <= nl) *(u32x4*)(out + (size_t)(rt * 64 + m * 16 + i) * 576 + (nt0 + n) * 16 + gp * 8) =
        u32x4{pk2(s0[0], s0[1]), pk2(s0[2], s0[3]), pk2(s1[0], s1[1]), pk2(s1[2], s1[3])};
  }
}

__global__ __launch_bounds__(512, 2) void k2_main(const u16* __restrict__ x, const u16* __restrict__ w_in,
                                                  const u16* __restrict__ wo, const u16* __restrict__ w_out,
                                                  const u16* __restrict__ w_sp, const u16* __restrict__ b_sp,
                                                  char* __restrict__ ws, u16* __restrict__ out) {
  __shared__ __attribute__((aligned(1024))) char sm[163840];
  char* kvs = sm + 20480;
  float* part = (float*)(sm + 72704);
  unsigned* obuf = (unsigned*)(sm + 154624);
  float* red = (float*)sm;
  unsigned* cnt = (unsigned*)(sm + 1024);
  const __amdgpu_buffer_rsrc_t rs_ = __builtin_amdgcn_make_buffer_rsrc(ws, 0, (unsigned)WS_END, 0x00020000);
  const int tid = threadIdx.x, lane = tid & 63;
  const int w = __builtin_amdgcn_readfirstlane(tid >> 6);
  const int b = blockIdx.x;
  const int r = lane & 31, h = lane >> 5;
  const int qh = w & 1, ksp = w >> 1;
  STAMP0(); STAMP(1, 0);
  const unsigned l16 = lane * 16, l8 = lane * 8;
  const int ntile = w < 4 ? 20 : 12;
  const char* kvu = ws + WS_KV + (size_t)(w < 4 ? ksp * 20 : 16 + ksp * 12) * 3584;
#define KV0(t, o) (*(const bfx8*)(kvu + ((t) * 3584 + (o)) + l16))
#define KV1(t) (*(const s16x4*)(kvu + ((t) * 3584 + 3072) + l8))
  bfx8 q0[4]; u32x2 q1[4]; unsigned qd[4];
  bfx8 kA; s16x4 kA1;
  auto ldq = [&]() {
#pragma unroll
  for (int a = 0; a < 4; a++) {
    const char* qp = ws + WS_Q + ((size_t)(b * 8 + qh * 4 + a) * 576 + r * 18) * 2;
    q0[a] = __builtin_bit_cast(bfx8, (u32x4) * (const u32x4_a4*)(qp + 16 * h));
    qd[a] = *(const unsigned*)(qp + 32);
  }
  kA = KV0(0, 0); kA1 = KV1(0);
  };
  const int xcd = b & 7, ii = b >> 3;
  const int rt = (xcd & 1) * 8 + (ii & 7), ct = (xcd >> 1) * 4 + (ii >> 3);
  float og[2], bs[2], gt2[2];
  if (w < 4) {
    ldq();
#pragma unroll
    for (int m = 0; m < 2; m++) {
      const int t = rt * 128 + w * 32 + m * 16 + (lane & 15);
      og[m] = ((const float*)(ws + WS_G))[t];
      bs[m] = __uint_as_float((unsigned)b_sp[t]);
      gt2[m] = ((const float*)(ws + WS_G))[b * 8 + w + 4 * m];
    }
    SCHED0();
  }
  if (tid < 4) cnt[tid] = 0u;
  WAITL0(); BAR();
  if (w >= 4) {
    const int pc0 = (w & 1) * 70 + ((w >> 1) & 1);
#pragma unroll
    for (int j = 0; j < 35; j++) glds16(ws + WS_KV + (pc0 + 2 * j) * 1024 + l16, kvs + (pc0 + 2 * j) * 1024);
  }
  if (w >= 4) {
    const int hw = w - 4, i = lane & 15, g = lane >> 4;
    __builtin_amdgcn_s_setprio(3);
    WAITV(28);
    if (lane == 0) asm volatile("ds_add_u32 %0, %1" ::"v"((unsigned)(uintptr_t)(LDSP unsigned*)&cnt[1]), "v"(1u) : "memory");
    WAITV(0);
    if (lane == 0) __hip_atomic_fetch_add(&cnt[2], 1u, __ATOMIC_RELAXED, __HIP_MEMORY_SCOPE_WORKGROUP);
    char* zu = ws + WS_Z2 + (size_t)b * 256 + (size_t)hw * 36 * 65536;
    const unsigned zo = i * 16 + g * 65536;
    u32x4 zr[9];
#pragma unroll
    for (int c = 0; c < 9; c++) zr[c] = *(const u32x4*)(zu + c * 4 * 65536 + zo);
    u32x2 wv_[2][8];
    unsigned wdst[2], wstr[2];
    const int job0 = hw * 256 + b;
#pragma unroll
    for (int u = 0; u < 2; u++) {
      const int job = u == 0 ? job0 : (job0 < 434 ? job0 + 1024 : job0);
      const u16* wsrc; int wld, whi = 4;
      if (job < 648) {
        int kt = job / 36, cb = job % 36;
        wsrc = w_in + (size_t)(kt * 32 + g * 8) * 4608 + cb * 64 + i * 4; wld = 4608;
        wdst[u] = (unsigned)WS_W1 + (cb * 4 * 18 + kt) * 1024; wstr[u] = 18 * 1024;
      } else {
        int j2 = job - 648, kt = j2 / 9, cb = j2 % 9;
        int c2 = kt - 18, pp = c2 / 9, c9 = c2 % 9;
        int f0 = pp * 288 + (c9 < 5 ? c9 * 32 : 144 + (c9 - 5) * 32) + 4 * g;
        if (kt >= 18) whi = c9 == 4 ? 144 : 16;
        wsrc = (kt < 18 ? wo + (size_t)(kt * 32 + g * 8) * 576 : w_out + (size_t)f0 * 576) + cb * 64 + i * 4;
        wld = 576;
        wdst[u] = (unsigned)WS_WO + (cb * 4 * 90 + kt) * 1024; wstr[u] = 90 * 1024;
      }
#pragma unroll
      for (int j = 0; j < 8; j++) wv_[u][j] = *(const u32x2*)(wsrc + (size_t)(j < 4 ? j : j - 4 + whi) * wld);
    }
    const char* pf = (const char*)w_sp + (size_t)b * 32768 + (tid - 256) * 64;
    const unsigned pf0 = *(const unsigned*)pf, pf1 = *(const unsigned*)(pf + 16384);
    SCHED0();
    float s1[8], s2[8];
#pragma unroll
    for (int j = 0; j < 8; j++) s1[j] = s2[j] = 0.f;
#pragma unroll
    for (int c = 0; c < 9; c++)
#pragma unroll
      for (int j = 0; j < 4; j++) {
        float a = bflo(zr[c][j]), bb = bfhi(zr[c][j]);
        s1[2 * j] += a; s2[2 * j] = __builtin_fmaf(a, a, s2[2 * j]);
        s1[2 * j + 1] += bb; s2[2 * j + 1] = __builtin_fmaf(bb, bb, s2[2 * j + 1]);
      }
#pragma unroll
    for (int j = 0; j < 8; j++) {
      s1[j] = dpp_add<0x121>(dpp_add<0x122>(dpp_add<0x124>(dpp_add<0x128>(s1[j]))));
      s2[j] = dpp_add<0x121>(dpp_add<0x122>(dpp_add<0x124>(dpp_add<0x128>(s2[j]))));
    }
    if (i == 0) {
      float* p = red + (hw * 4 + g) * 16;
      *(f32x4*)p = f32x4{s1[0], s1[1], s1[2], s1[3]}; *(f32x4*)(p + 4) = f32x4{s1[4], s1[5], s1[6], s1[7]};
      *(f32x4*)(p + 8) = f32x4{s2[0], s2[1], s2[2], s2[3]}; *(f32x4*)(p + 12) = f32x4{s2[4], s2[5], s2[6], s2[7]};
    }
    WAITL0();
    if (lane == 0) __hip_atomic_fetch_add(&cnt[0], 1u, __ATOMIC_RELAXED, __HIP_MEMORY_SCOPE_WORKGROUP);
#pragma unroll
    for (int u = 0; u < 2; u++) {
#pragma unroll
      for (int j = 0; j < 8; j++) PIN(wv_[u][j]);
      if (u == 0 || job0 < 434) {
#pragma unroll
        for (int c = 0; c < 2; c++) {
          u32x4 lo, hi;
#pragma unroll
          for (int j = 0; j < 4; j++) {
            lo[j] = __builtin_amdgcn_perm(wv_[u][2 * j + 1][c], wv_[u][2 * j][c], 0x05040100u);
            hi[j] = __builtin_amdgcn_perm(wv_[u][2 * j + 1][c], wv_[u][2 * j][c], 0x07060302u);
          }
          char* bq = sm + 2048 + hw * 4096 + (i >> 2) * 1024 + ((i & 3) * 4 + g * 16) * 16 + c * 32;
          *(u32x4*)bq = lo; *(u32x4*)(bq + 16) = hi;
        }
        u32x4 fv[4];
#pragma unroll
        for (int c = 0; c < 4; c++) fv[c] = *(const u32x4*)(sm + 2048 + hw * 4096 + c * 1024 + l16);
#pragma unroll
        for (int c = 0; c < 4; c++) ST16(fv[c], wdst[u] + c * wstr[u] + l16);
      }
    }
    asm volatile("" ::"v"(pf0), "v"(pf1));
    POLL4(cnt[0]);
    float t = 0.f;
#pragma unroll
    for (int j = 0; j < 4; j++) t += red[j * 64 + lane];
    t += __shfl_xor(t, 16, 64); t += __shfl_xor(t, 32, 64);
    float t2 = __builtin_bit_cast(float, __builtin_amdgcn_update_dpp(0, __builtin_bit_cast(int, t), 0x128, 0xf, 0xf, true));
    float mean = t * (1.0f / 2304.0f);
    float var = fmaxf(t2 * (1.0f / 2304.0f) - mean * mean, 0.f);
    float rsv = __builtin_amdgcn_rsqf(var + 1e-5f), muv = -mean * rsv;
    float rs[8], mu[8];
#pragma unroll
    for (int j = 0; j < 8; j++) {
      rs[j] = __builtin_bit_cast(float, __builtin_amdgcn_readlane(__builtin_bit_cast(int, rsv), j));
      mu[j] = __builtin_bit_cast(float, __builtin_amdgcn_readlane(__builtin_bit_cast(int, muv), j));
    }
#pragma unroll
    for (int c = 0; c < 9; c++) {
      u32x4 o;
#pragma unroll
      for (int j = 0; j < 4; j++)
        o[j] = pk2(__builtin_fmaf(bflo(zr[c][j]), rs[2 * j], mu[2 * j]), __builtin_fmaf(bfhi(zr[c][j]), rs[2 * j + 1], mu[2 * j + 1]));
      ST16(o, (unsigned)WS_ZN + b * 256 + (hw * 36 + c * 4) * 65536 + zo);
    }
    SCHED0();
    ldq();
  } else {
    PIN(og[0]); PIN(og[1]); PIN(bs[0]); PIN(bs[1]);
  }
  STAMP(1, 1);
  bfx8 vA0, vA1;
  f32x16 O[4], sA, sB;
  float mrow[4];
  u32x4 pX, pY;
  bfx8 kB, vB0, vB1;
  s16x4 kB1;
  const f32x16 zero16 = {0.f, 0.f, 0.f, 0.f, 0.f, 0.f, 0.f, 0.f, 0.f, 0.f, 0.f, 0.f, 0.f, 0.f, 0.f, 0.f};
#pragma unroll
  for (int a = 0; a < 4; a++) qd[a] = h == 0 ? qd[a] : 0u;
#pragma unroll
  for (int a = 0; a < 4; a++) {
    f32x16 s = MFMA32(kA, q0[a], zero16);
    s = MFMA32H(kA1, __builtin_bit_cast(s16x4, u32x2{qd[a], 0u}), s);
    float m = s[0];
#pragma unroll
    for (int j = 1; j < 16; j++) m = fmaxf(m, s[j]);
    mrow[a] = m + 64.0f;
    PIN(mrow[a]);
  }
  auto setup = [&]() {
#pragma unroll
    for (int a = 0; a < 4; a++) {
      auto sw = __builtin_amdgcn_permlane32_swap(__float_as_uint(mrow[a]), __float_as_uint(mrow[a]), false, false);
      float m = fmaxf(__uint_as_float(sw[0]), __uint_as_float(sw[1]));
      mrow[a] = m;
      float nm = -m;
      unsigned hi = __float_as_uint(nm) & 0xffff0000u;
      float r1 = nm - __uint_as_float(hi);
      unsigned mid = __float_as_uint(r1) & 0xffff0000u;
      float r2 = r1 - __uint_as_float(mid);
      unsigned lo = __float_as_uint(r2) & 0xffff0000u;
      q1[a] = h == 0 ? u32x2{qd[a], (hi >> 16) | mid} : u32x2{lo >> 16, 0u};
      O[a] = zero16;
    }
    sA = MFMA32(kA, q0[0], zero16);
    sA = MFMA32H(kA1, __builtin_bit_cast(s16x4, q1[0]), sA);
    pX = u32x4{0u, 0u, 0u, 0u};
    vB1 = vA0;
    SCHED0();
  };
  const char* kvl = kvs + ksp * 20 * 3584;
#define KL0(t, o) (LD ? *(const bfx8*)(kvl + ((t) * 3584 + (o)) + l16) : KV0(t, o))
#define KL1(t) (LD ? *(const s16x4*)(kvl + ((t) * 3584 + 3072) + l8) : KV1(t))
  auto pair = [&](int t, auto ldc) {
    constexpr bool LD = decltype(ldc)::value;
    kB = KL0(t + 1, 0); kB1 = KL1(t + 1); vB0 = KL0(t + 1, 1024);
    SCHED0();
    ATT_STEP(sA, sB, kA, kA1, q0[1], q1[1], O[0], vA0, O[3], vB1, pX, pY);
    vB1 = KL0(t + 1, 2048);
    SCHED0();
    ATT_STEP(sB, sA, kA, kA1, q0[2], q1[2], O[1], vA0, O[0], vA1, pY, pX);
    ATT_STEP(sA, sB, kA, kA1, q0[3], q1[3], O[2], vA0, O[1], vA1, pX, pY);
    ATT_STEP(sB, sA, kB, kB1, q0[0], q1[0], O[3], vA0, O[2], vA1, pY, pX);
    const int t2 = t + 2 < ntile ? t + 2 : t + 1;
    kA = KL0(t2, 0); kA1 = KL1(t2); vA0 = KL0(t2, 1024);
    SCHED0();
    ATT_STEP(sA, sB, kB, kB1, q0[1], q1[1], O[0], vB0, O[3], vA1, pX, pY);
    vA1 = KL0(t2, 2048);
    SCHED0();
    ATT_STEP(sB, sA, kB, kB1, q0[2], q1[2], O[1], vB0, O[0], vB1, pY, pX);
    ATT_STEP(sA, sB, kB, kB1, q0[3], q1[3], O[2], vB0, O[1], vB1, pX, pY);
    ATT_STEP(sB, sA, kA, kA1, q0[0], q1[0], O[3], vB0, O[2], vB1, pY, pX);
  };
  for (int attempt = 0; attempt < 2; attempt++) {
    if (w < 4) { POLL4(cnt[1]); vA0 = *(const bfx8*)(kvl + 1024 + l16); vA1 = *(const bfx8*)(kvl + 2048 + l16); SCHED0(); }
    else {
      vA0 = KV0(0, 1024); vA1 = KV0(0, 2048);
      if (attempt == 0) {
        WAITV(0);
        if (lane == 0 && __hip_atomic_fetch_add(&cnt[3], 1u, __ATOMIC_RELAXED, __HIP_MEMORY_SCOPE_WORKGROUP) == 3u) SIGNAL(0);
      }
    }
    setup();
    STAMP(1, 2);
    if (w < 4) { for (int t = 0; t < 20; t += 2) { if (t == 2) POLL4(cnt[2]); pair(t, BoolC<true>{}); } }
    else {
      __builtin_amdgcn_s_setprio(BPRIO);
      for (int t = 0; t < 12; t += 2) pair(t, BoolC<false>{});
    }
    O[3] = MFMA32(vB1, __builtin_bit_cast(bfx8, pX), O[3]);
    float chk = 0.f, lmax = 0.f;
#pragma unroll
    for (int a = 0; a < 4; a++) {
#pragma unroll
      for (int j = 0; j < 11; j++) chk += fabsf(O[a][j]);
      lmax = fmaxf(lmax, h == 0 ? O[a][10] : O[a][8]);
    }
    bool bad = !(lmax < 1.0e33f) || !(chk < 3.0e38f);
    if (attempt == 1 || !__any(bad)) break;
#pragma unroll
    for (int a = 0; a < 4; a++) mrow[a] = -INFINITY;
    for (int t = 0; t < ntile; t++) {
      bfx8 k0 = KV0(t, 0);
      s16x4 k1 = KV1(t);
#pragma unroll
      for (int a = 0; a < 4; a++) {
        f32x16 s = MFMA32(k0, q0[a], zero16);
        s = MFMA32H(k1, __builtin_bit_cast(s16x4, u32x2{qd[a], 0u}), s);
#pragma unroll
        for (int j = 0; j < 16; j++) mrow[a] = fmaxf(mrow[a], s[j]);
      }
    }
    kA = KV0(0, 0); kA1 = KV1(0);
  }
  STAMP(1, 3);
  __builtin_amdgcn_s_setprio(0);
  if (w == ACQW) { AWAIT(0, 256u); __builtin_amdgcn_fence(__ATOMIC_ACQUIRE, "agent"); WAITV(0); }
  WAITL0(); BAR();
#pragma unroll
  for (int a = 0; a < 4; a++) {
    float* p = part + ((ksp * 8 + qh * 4 + a) * 32 + r) * 20;
    *(f32x4*)(p + 4 * h) = f32x4{O[a][0], O[a][1], O[a][2], O[a][3]};
    *(f32x4*)(p + 8 + 4 * h) = f32x4{O[a][4], O[a][5], O[a][6], O[a][7]};
    if (h == 0) *(f32x4*)(p + 16) = f32x4{O[a][8], O[a][9], O[a][10], mrow[a]};
  }
  WAITL0(); BAR();
  const int rt4 = xcd * 4 + (ii & 3), cg = ii >> 2;
  const int nt0 = (cg >> 1) * 9 + (cg & 1) * 5, nl4 = 4 - (cg & 1);
  const u32x4 rsv = {(unsigned)(size_t)ws, (unsigned)((size_t)ws >> 32) & 0xffffu, (unsigned)WS_END, 0x00020000u};
  f32x4 acc4[4][5];
  bfx8 Bf4[3][5];
  const char* bp4 = ws + WS_WO + (size_t)nt0 * 90 * 1024 + lane * 16;
  auto ldb4 = [&](int buf, int k) {
#pragma unroll
    for (int n = 0; n < 5; n++) ALD16(Bf4[buf][n], lane * 16, (int)WS_WO + ((nt0 + (n < 4 ? n : nl4)) * 90 + k) * 1024, "");
  };
  auto pinb = [&]() {
#pragma unroll
    for (int q = 0; q < 15; q++) PIN(Bf4[q / 5][q % 5]);
  };
  if (w >= 4) {
    const int lw = w - 4, r8 = lane >> 3, p = lane & 7;
    const int nb = lw < 2 ? 5 : 4, b0 = lw < 2 ? lw * 5 : 10 + (lw - 2) * 4;
    const char* a1 = (const char*)x + (size_t)(rt * 128 + lw * 32 + r8) * 1152 + (p ^ r8) * 16;
    const char* a2 = (const char*)w_sp + (size_t)(rt * 128 + lw * 32 + r8) * 4096 + (p ^ r8) * 16;
    const char* b1 = ws + WS_W1 + (size_t)ct * 9 * 18 * 1024 + lane * 16;
    const char* b2 = ws + WS_ZN + (size_t)ct * 9 * 64 * 1024 + lane * 16;
    auto issue = [&](int j) {
      char* st = sm + (j & 3) * K3_ST;
      if (j < 9) {
#pragma unroll
        for (int u = 0; u < 4; u++) glds16(a1 + u * 8 * 1152 + j * 128, st + (lw * 4 + u) * 1024);
#pragma unroll
        for (int u = 0; u < 5; u++)
          if (u < nb) { int pc = b0 + u; glds16(b1 + ((pc >> 1) * 18 + 2 * j + (pc & 1)) * 1024, st + 16384 + pc * 1024); }
      } else {
        const int kk = j - 9;
#pragma unroll
        for (int u = 0; u < 4; u++) glds16(a2 + u * 8 * 4096 + kk * 128, st + (lw * 4 + u) * 1024);
#pragma unroll
        for (int u = 0; u < 5; u++)
          if (u < nb) { int pc = b0 + u; glds16(b2 + ((pc >> 1) * 64 + 2 * kk + (pc & 1)) * 1024, st + 16384 + pc * 1024); }
      }
    };
    __builtin_amdgcn_s_waitcnt(0x0F70);
    issue(0); issue(1);
    BAR();
    if (lw < 2) WAITV(9); else WAITV(8);
    BAR();
    issue(2); issue(3);
    for (int j = 0; j < 18; j++) {
      if (lw < 2) WAITV(18); else WAITV(16);
      BAR();
      issue(j + 4);
    }
    bfx8 Af[3][4];
    auto lda = [&](int buf, int k) {
#pragma unroll
      for (int m = 0; m < 4; m++) ALD16(Af[buf][m], lane * 16, (int)WS_AH + ((rt4 * 4 + m) * 90 + k) * 1024, " sc1");
    };
    auto mm = [&](int buf) {
#pragma unroll
      for (int m = 0; m < 4; m++) PIN(Af[buf][m]);
#pragma unroll
      for (int n = 0; n < 5; n++) PIN(Bf4[buf][n]);
#pragma unroll
      for (int m = 0; m < 4; m++)
#pragma unroll
        for (int n = 0; n < 5; n++) acc4[m][n] = MFMA16(Bf4[buf][n], Af[buf][m], acc4[m][n]);
    };
    auto stage = [&](int i0, int i1) {
#pragma unroll
      for (int i2 = i0; i2 < i1; i2++) {
        const int sl = lw + 4 * i2, wk = sl / 5, n = sl - wk * 5, ww = wk / 3, k = wk - ww * 3;
        glds16(bp4 + (n < nl4 ? n : nl4) * 90 * 1024 + (21 + ww * 9 + k) * 1024, sm + K3_ST + sl * 1024);
      }
    };
#pragma unroll
    for (int m = 0; m < 4; m++)
#pragma unroll
      for (int n = 0; n < 5; n++) acc4[m][n] = f32x4{0.f, 0.f, 0.f, 0.f};
#define IT(j, X0, X1) SCHED0(); if (lw < 2) WAITV(X0); else WAITV(X1); BAR(); if ((j) + 4 < 41) issue((j) + 4); SCHED0();
    IT(18, 18, 16);
    unsigned oc;
    asm volatile("buffer_load_dword %0, off, %1, %2 sc1" : "=v"(oc) : "s"(rsv), "s"((int)WS_CN + (1 + rt4) * 128) : "memory");
    IT(19, 19, 17); IT(20, 19, 17); IT(21, 19, 17); IT(22, 18, 16);
    PIN(oc);
    if (__builtin_amdgcn_readfirstlane(oc) < 8u) AWAIT(1 + rt4, 8u);
    asm volatile("" ::: "memory");
    lda(0, b0); ldb4(0, b0);
    IT(23, 27, 25); lda(1, b0 + 1); ldb4(1, b0 + 1);
    IT(24, 36, 34); lda(2, b0 + 2); ldb4(2, b0 + 2);
    IT(25, 45, 43);
    IT(26, 36, 34); mm(0); SCHED0(); lda(0, b0 + 3); ldb4(0, b0 + 3);
    IT(27, 36, 34); mm(1); SCHED0(); lda(1, b0 + 4); ldb4(1, b0 + 4);
    IT(28, 36, 34); mm(2);
    IT(29, 36, 34);
    IT(30, 27, 25); mm(0);
    IT(31, 18, 16); if (lw < 2) mm(1);
    IT(32, 18, 16);
    IT(33, 18, 16); ldb4(0, 18 + w * 9);
    IT(34, 23, 21); ldb4(1, 19 + w * 9);
    IT(35, 28, 26); ldb4(2, 20 + w * 9);
    IT(36, 33, 31); stage(26, 30);
    IT(37, 32, 30); stage(0, 8);
    IT(38, 26, 25); stage(8, 17);
    IT(39, 21, 21); stage(17, 26);
    SCHED0(); BAR();
    WAITV(0);
#undef IT
    STAMP(1, 4); STAMP(1, 5); STAMP(1, 6);
    pinb();
  } else {
    const int i = lane & 15, g = lane >> 4;
    og[0] = 1.0f - og[0]; og[1] = 1.0f - og[1];
    bs[0] = bflo(__float_as_uint(bs[0])); bs[1] = bflo(__float_as_uint(bs[1]));
#pragma unroll
    for (int u = 0; u < 2; u++) {
      const int tok = w + 4 * u;
      const float gt = gt2[u];
      const float* p = part + (tok * 32 + r) * 20;
      f32x4 tl[4]; float L[4], M = -INFINITY;
#pragma unroll
      for (int k = 0; k < 4; k++) {
        tl[k] = *(const f32x4*)(p + k * 5120 + 16);
        L[k] = tl[k][3] + __builtin_amdgcn_logf(tl[k][2]);
        M = fmaxf(M, L[k]);
      }
      float den = 0.f;
#pragma unroll
      for (int k = 0; k < 4; k++) { L[k] = EX2(L[k] - M); den += L[k]; }
      const float inv = gt * __builtin_amdgcn_rcpf(den);
      f32x4 o0 = {0.f, 0.f, 0.f, 0.f}, o1 = o0; float o2 = 0.f, o3 = 0.f;
#pragma unroll
      for (int k = 0; k < 4; k++) {
        float sc = L[k] * inv * __builtin_amdgcn_rcpf(tl[k][2]);
        o0 += *(const f32x4*)(p + k * 5120 + 8 * h) * sc;
        o1 += *(const f32x4*)(p + k * 5120 + 8 * h + 4) * sc;
        o2 += tl[k][0] * sc; o3 += tl[k][1] * sc;
      }
      unsigned* ob = obuf + tok * 288 + r * 9 + 4 * h;
      ob[0] = pk2(o0[0], o0[1]); ob[1] = pk2(o0[2], o0[3]); ob[2] = pk2(o1[0], o1[1]); ob[3] = pk2(o1[2], o1[3]);
      if (h == 0) ob[8] = pk2(o2, o3);
    }
    WAITL0(); BAR();
    unsigned* cnt2 = (unsigned*)(sm + 160000);
    {
      u32x4 ov[3];
#pragma unroll
      for (int u = 0; u < 3; u++) { int c = tid + u * 256; c = c < 576 ? c : 575; ov[u] = *(const u32x4*)(obuf + (c & 7) * 288 + (c >> 3) * 4); }
#pragma unroll
      for (int u = 0; u < 3; u++) {
        const int c = tid + u * 256, ch = c >> 3, t = b * 8 + (c & 7);
        if (u < 2 || w == 0) ST16(ov[u], (unsigned)WS_AH + ((t >> 4) * 90 + (ch >> 2)) * 1024 + ((t & 15) + 16 * (ch & 3)) * 16);
      }
    }
    f32x4 acc[9][2], z1[9][2];
    unsigned z1p[9][2][2];
#pragma unroll
    for (int n = 0; n < 9; n++) acc[n][0] = acc[n][1] = f32x4{0.f, 0.f, 0.f, 0.f};
    const int aoff0 = (w * 32 + i) * 128 + ((g ^ (i & 7)) * 16), aoff1 = aoff0 ^ 64;
    const int boff = 16384 + lane * 16;
    bfx8 Af[2][2], Bf[2][9];
    auto rd = [&](int buf, char* st, int ks) {
#pragma unroll
      for (int m = 0; m < 2; m++) Af[buf][m] = *(const bfx8*)(st + (ks ? aoff1 : aoff0) + m * 2048);
#pragma unroll
      for (int n = 0; n < 9; n++) Bf[buf][n] = *(const bfx8*)(st + boff + (n * 2 + ks) * 1024);
    };
    auto mm = [&](int buf) {
#pragma unroll
      for (int n = 0; n < 9; n++)
#pragma unroll
        for (int m = 0; m < 2; m++) acc[n][m] = MFMA16(Bf[buf][n], Af[buf][m], acc[n][m]);
    };
    auto ilv = [&](bool gl) {
#pragma unroll
      for (int x2 = 0; x2 < 18; x2++) {
        __builtin_amdgcn_sched_group_barrier(0x8, 1, 0);
        if (x2 < 11) __builtin_amdgcn_sched_group_barrier(0x100, 1, 0);
        if (gl) __builtin_amdgcn_sched_group_barrier(0x402, 1, 0);
      }
    };
    auto body = [&](int j, int gi) {
      int so0 = (j & 3) * K3_ST, so1 = ((j + 1) & 3) * K3_ST;
      asm volatile("" : "+s"(so0), "+s"(so1));
      if (gi >= 0) PIN(z1[gi >> 1][gi & 1]);
      rd(1, sm + so0, 1); mm(0);
      if (gi >= 0) { f32x4 v = z1[gi >> 1][gi & 1]; z1p[gi >> 1][gi & 1][0] = pk2(gelu_t(v[0]) * og[gi & 1], gelu_t(v[1]) * og[gi & 1]); }
      ilv(gi >= 0);
      SCHED0();
      if (gi >= 0) { PIN(z1p[gi >> 1][gi & 1][0]); PIN(z1[gi >> 1][gi & 1]); }
      WAITL0(); BAR(); SCHED0();
      rd(0, sm + so1, 0);
      mm(1);
      if (gi >= 0) { f32x4 v = z1[gi >> 1][gi & 1]; z1p[gi >> 1][gi & 1][1] = pk2(gelu_t(v[2]) * og[gi & 1], gelu_t(v[3]) * og[gi & 1]); }
      ilv(gi >= 0);
      SCHED0();
      if (gi >= 0) PIN(z1p[gi >> 1][gi & 1][1]);
    };
    WAITL0(); BAR(); SCHED0();
    STAMP(1, 4);
    rd(0, sm, 0);
    SCHED0();
    for (int j = 0; j < 8; j++) body(j, -1);
    WAITV(0);
    body(8, -1);
    if (tid == 0) { SIGNAL(1 + (b >> 3)); *cnt2 = 0u; }
#pragma unroll
    for (int n = 0; n < 9; n++)
#pragma unroll
      for (int m = 0; m < 2; m++) { z1[n][m] = acc[n][m]; acc[n][m] = f32x4{0.f, 0.f, 0.f, 0.f}; }
    SCHED0();
#pragma unroll
    for (int j = 9; j < 27; j++) body(j, j - 9);
    for (int j = 27; j < 41; j++) body(j, -1);
    STAMP(1, 5);
    SCHED0(); ldb4(0, 18 + w * 9); ldb4(1, 19 + w * 9); ldb4(2, 20 + w * 9); SCHED0();
#pragma unroll
    for (int m = 0; m < 2; m++) {
      const unsigned dst = (unsigned)WS_AH + ((rt * 8 + w * 2 + m) * 90 + 18 + (ct >> 1) * 9) * 1024 + lane * 16;
      unsigned hp[9][2];
#pragma unroll
      for (int n = 0; n < 9; n++) {
        f32x4 a = acc[n][m];
        hp[n][0] = pk2(bflo(z1p[n][m][0]) * (a[0] + bs[m]), bfhi(z1p[n][m][0]) * (a[1] + bs[m]));
        hp[n][1] = pk2(bflo(z1p[n][m][1]) * (a[2] + bs[m]), bfhi(z1p[n][m][1]) * (a[3] + bs[m]));
        if (n & 1) ST16((u32x4{hp[n - 1][0], hp[n - 1][1], hp[n][0], hp[n][1]}), dst + ((ct & 1) * 5 + (n >> 1)) * 1024);
      }
      ST8((u32x2{hp[8][0], hp[8][1]}), dst + 4 * 1024 + (ct & 1) * 8);
    }
    WAITV(0);
    pinb();
    if (lane == 0 && __hip_atomic_fetch_add(cnt2, 1u, __ATOMIC_RELAXED, __HIP_MEMORY_SCOPE_WORKGROUP) == 3u) SIGNAL(33 + rt);
    STAMP(1, 6);
#pragma unroll
    for (int m = 0; m < 4; m++)
#pragma unroll
      for (int n = 0; n < 5; n++) acc4[m][n] = f32x4{0.f, 0.f, 0.f, 0.f};
  }
  {
    k4_body(sm, ws, rsv, out, rt4, nt0, nl4, w, lane, acc4, Bf4 STARG);
  }
  STAMPE(1, 7);
}

extern "C" void kernel_launch(void* const* d_in, const int* in_sizes, int n_in, void* d_out, int out_size,
                              void* d_ws, size_t ws_size, hipStream_t stream) {
  const u16* x = (const u16*)d_in[0];
  const u16* wq = (const u16*)d_in[1];
  const u16* wk = (const u16*)d_in[2];
  const u16* wv = (const u16*)d_in[3];
  const u16* wo = (const u16*)d_in[4];
  const u16* w_in = (const u16*)d_in[6];
  const u16* w_sp = (const u16*)d_in[7];
  const u16* b_sp = (const u16*)d_in[8];
  const u16* w_out = (const u16*)d_in[9];
  const u16* gw = (const u16*)d_in[10];
  char* ws = (char*)d_ws;
  k1_proj<<<256, 512, 0, stream>>>(x, wq, wk, wv, w_in, gw, ws);
  k2_main<<<256, 512, 0, stream>>>(x, w_in, wo, w_out, w_sp, b_sp, ws, (u16*)d_out);
}
```

```cpp
#include <hip/hip_runtime.h>
#include <stdint.h>

typedef __attribute__((ext_vector_type(8))) __bf16 bfx8;
typedef __attribute__((ext_vector_type(2))) __bf16 bfx2;
typedef __attribute__((ext_vector_type(4))) short s16x4;
typedef __attribute__((ext_vector_type(4))) float f32x4;
typedef __attribute__((ext_vector_type(16))) float f32x16;
typedef __attribute__((ext_vector_type(4))) unsigned u32x4;
typedef __attribute__((ext_vector_type(2))) unsigned u32x2;
typedef u32x4 __attribute__((aligned(4))) u32x4_a4;
typedef unsigned short u16;
#define LDSP __attribute__((address_space(3)))
#define GLBP __attribute__((address_space(1)))

constexpr int S_ = 2048, D_ = 576, FH = 2304;
constexpr size_t WS_Q   = 0;
constexpr size_t WS_KV  = WS_Q + (size_t)S_ * D_ * 2;
constexpr size_t WS_G   = WS_KV + 64 * 3584;
constexpr size_t WS_Z2  = WS_G + 2048 * 4;
constexpr size_t WS_W1  = WS_Z2 + (size_t)144 * 64 * 1024;
constexpr size_t WS_WO  = WS_W1 + (size_t)144 * 18 * 1024;
constexpr size_t WS_AH  = WS_WO + (size_t)36 * 90 * 1024;
constexpr size_t WS_ZN  = WS_AH + (size_t)128 * 90 * 1024;
constexpr size_t WS_CN  = WS_ZN + (size_t)144 * 64 * 1024;
constexpr size_t WS_END = WS_CN + 64 * 128;
typedef GLBP unsigned gu32;

__device__ __forceinline__ unsigned pk2(float a, float b) { bfx2 r; r[0] = (__bf16)a; r[1] = (__bf16)b; return __builtin_bit_cast(unsigned, r); }
__device__ __forceinline__ float bflo(unsigned u) { return __uint_as_float(u << 16); }
__device__ __forceinline__ float bfhi(unsigned u) { return __uint_as_float(u & 0xffff0000u); }
__device__ __forceinline__ float gelu_t(float x) {
  const float c0 = -2.302208198f;
  const float c1 = c0 * 0.044715f;
  float e = __builtin_amdgcn_exp2f(x * __builtin_fmaf(x * x, c1, c0));
  return x * __builtin_amdgcn_rcpf(1.0f + e);
}
__device__ __forceinline__ void glds16(const void* g, char* l) {
  __builtin_amdgcn_global_load_lds((const GLBP void*)g, (LDSP void*)l, 16, 0, 0);
}
#define ST16(v, off) __builtin_amdgcn_raw_buffer_store_b128(v, rs_, (unsigned)(off), 0, 16)
#define ST8(v, off) __builtin_amdgcn_raw_buffer_store_b64(v, rs_, (unsigned)(off), 0, 16)
#define LD16(off, so) __builtin_amdgcn_raw_buffer_load_b128(rs_, (unsigned)(off), so, 16)
#define SIGNAL(c) __hip_atomic_fetch_add((gu32*)(ws + WS_CN + (c) * 128), 1u, __ATOMIC_RELAXED, __HIP_MEMORY_SCOPE_AGENT)
#define AWAIT(c, n) do { while (__hip_atomic_load((gu32*)(ws + WS_CN + (c) * 128), __ATOMIC_RELAXED, __HIP_MEMORY_SCOPE_AGENT) < (n)) \
  __builtin_amdgcn_s_sleep(4); asm volatile("" ::: "memory"); } while (0)
#define MFMA16(a, b, c) __builtin_amdgcn_mfma_f32_16x16x32_bf16(a, b, c, 0, 0, 0)
#define MFMA32(a, b, c) __builtin_amdgcn_mfma_f32_32x32x16_bf16(a, b, c, 0, 0, 0)
#define MFMA32H(a, b, c) __builtin_amdgcn_mfma_f32_32x32x8bf16_1k(a, b, c, 0, 0, 0)
#define WAITV(n) asm volatile("s_waitcnt vmcnt(" #n ")" ::: "memory")
#define WAITL0() asm volatile("s_waitcnt lgkmcnt(0)" ::: "memory")
#define BAR() __builtin_amdgcn_s_barrier()
#define SCHED0() __builtin_amdgcn_sched_barrier(0)
#define STPARM
#define STARG
#define STAMP0()
#define STAMP(k, i)
#define STAMPE(k, n)

constexpr int K1_ST = 40960, K1_NS = 3;

template <bool ISQ>
__device__ __forceinline__ void k1_consumer(char* sm, int w, int lane, int rt, int ct, char* ws) {
  const int wm = w & 1, wn = w >> 1, i = lane & 15, g = lane >> 4;
  f32x4 acc[4][6];
#pragma unroll
  for (int m = 0; m < 4; m++)
#pragma unroll
    for (int n = 0; n < 6; n++) acc[m][n] = f32x4{0.f, 0.f, 0.f, 0.f};
  const int arow = wm * 64 + 8 * (i >> 2) + (i & 3);
  const int aoff0 = arow * 128 + ((g ^ (i & 7)) * 16), aoff1 = aoff0 ^ 64;
  int boff[6];
  {
    const int q = i >> 2, p = i & 3, h = ((q >> 1) & 1) | ((g & 1) << 1);
#pragma unroll
    for (int n = 0; n < 6; n++) {
      int nt = wn * 6 + n;
      boff[n] = 16384 + (nt >> 2) * 8192 + g * 1024 + q * 128 + (((nt & 3) ^ h) * 32) + p * 8;
    }
  }
  bfx8 Af[2][4], Bf[2][6];
  auto rd = [&](int buf, char* st, int ks) {
#pragma unroll
    for (int n = 0; n < 6; n++) {
      s16x4 lo = __builtin_amdgcn_ds_read_tr16_b64_v4i16((LDSP s16x4*)(st + boff[n] + ks * 4096));
      s16x4 hi = __builtin_amdgcn_ds_read_tr16_b64_v4i16((LDSP s16x4*)(st + boff[n] + ks * 4096 + 512));
      Bf[buf][n] = __builtin_bit_cast(bfx8, __builtin_shufflevector(lo, hi, 0, 1, 2, 3, 4, 5, 6, 7));
    }
#pragma unroll
    for (int m = 0; m < 4; m++)
      Af[buf][m] = *(const bfx8*)(st + (ks ? aoff1 : aoff0) + (4 * (m & 1) + 32 * (m >> 1)) * 128);
  };
  auto mm = [&](int buf) {
#pragma unroll
    for (int m = 0; m < 4; m++)
#pragma unroll
      for (int n = 0; n < 6; n++)
        acc[m][n] = ISQ ? MFMA16(Bf[buf][n], Af[buf][m], acc[m][n]) : MFMA16(Af[buf][m], Bf[buf][n], acc[m][n]);
  };
  auto ilv = [&]() {
#pragma unroll
    for (int x = 0; x < 8; x++) {
      __builtin_amdgcn_sched_group_barrier(0x8, 2, 0);
      __builtin_amdgcn_sched_group_barrier(0x100, 2, 0);
    }
    __builtin_amdgcn_sched_group_barrier(0x8, 8, 0);
  };
  STAMP0(); STAMP(0, 0);
  BAR(); SCHED0();
  STAMP(0, 1);
  rd(0, sm, 0);
  SCHED0();
#pragma unroll
  for (int kt = 0; kt < 9; kt++) {
    char* st = sm + (kt % K1_NS) * K1_ST;
    rd(1, st, 1); mm(0); ilv();
    SCHED0(); WAITL0(); BAR(); SCHED0();
    if (kt < 8) rd(0, sm + ((kt + 1) % K1_NS) * K1_ST, 0);
    mm(1);
    if (kt < 8) ilv();
    SCHED0();
  }
  STAMP(0, 2);
  if (!ISQ) {
    char* z2 = ws + WS_Z2;
#pragma unroll
    for (int n = 0; n < 6; n++)
#pragma unroll
      for (int mp = 0; mp < 2; mp++) {
        f32x4 a = acc[2 * mp][n], b = acc[2 * mp + 1][n];
        u32x4 o = {pk2(gelu_t(a[0]), gelu_t(a[1])), pk2(gelu_t(a[2]), gelu_t(a[3])),
                   pk2(gelu_t(b[0]), gelu_t(b[1])), pk2(gelu_t(b[2]), gelu_t(b[3]))};
        int ft = ct * 12 + wn * 6 + n, kt32 = rt * 4 + wm * 2 + mp;
        *(u32x4*)(z2 + ((size_t)(ft * 64 + kt32) * 64 + lane) * 16) = o;
      }
  } else {
    const float c = 0.34004649f;
    char* q = ws + WS_Q;
#pragma unroll
    for (int m = 0; m < 4; m++) {
      int t = rt * 128 + arow + 4 * (m & 1) + 32 * (m >> 1);
#pragma unroll
      for (int n = 0; n < 6; n++) {
        f32x4 a = acc[m][n];
        u32x2 o = {pk2(a[0] * c, a[1] * c), pk2(a[2] * c, a[3] * c)};
        *(u32x2*)(q + ((size_t)t * 576 + (ct - 12) * 192 + wn * 96 + n * 16 + g * 4) * 2) = o;
      }
    }
  }
  STAMPE(0, 3);
}

__global__ __launch_bounds__(512, 2) void k1_proj(const u16* __restrict__ x, const u16* __restrict__ wq,
                                                  const u16* __restrict__ wk, const u16* __restrict__ wv,
                                                  const u16* __restrict__ w_in, const u16* __restrict__ gw,
                                                  char* __restrict__ ws) {
  __shared__ __attribute__((aligned(1024))) char sm[K1_ST * K1_NS];
  const int tid = threadIdx.x, lane = tid & 63;
  const int w = __builtin_amdgcn_readfirstlane(tid >> 6);
  const int b = blockIdx.x, xcd = b & 7, ii = b >> 3;
  const int rt = (xcd & 1) * 8 + (ii & 7), ct = (xcd >> 1) * 4 + (ii >> 3);
  if (b == 0 && tid < 64) *(unsigned*)(ws + WS_CN + tid * 128) = 0u;
  if (ct == 15) {
    const int i = lane & 15, g = lane >> 4;
    bfx8 Af[18];
    const u16* xr = x + (size_t)(rt * 128 + w * 16 + i) * 576 + g * 8;
#pragma unroll
    for (int k = 0; k < 18; k++) Af[k] = *(const bfx8*)(xr + k * 32);
    for (int fr = w; fr < 54; fr += 8) {
      int nt = fr / 18, kt = fr % 18, n = nt * 16 + i, k0 = kt * 32 + g * 8;
      const u16* src = n < 18 ? wk + n : (n < 36 ? wv + (n - 18) : gw);
      int str = n < 36 ? 18 : 1;
      unsigned v[8];
#pragma unroll
      for (int j = 0; j < 8; j++) v[j] = src[(k0 + j) * str];
      u32x4 o = {v[0] | (v[1] << 16), v[2] | (v[3] << 16), v[4] | (v[5] << 16), v[6] | (v[7] << 16)};
      if (n > 36) o = u32x4{0, 0, 0, 0};
      *(u32x4*)(sm + fr * 1024 + lane * 16) = o;
    }
    __syncthreads();
    f32x4 acc[3] = {{0.f, 0.f, 0.f, 0.f}, {0.f, 0.f, 0.f, 0.f}, {0.f, 0.f, 0.f, 0.f}};
#pragma unroll
    for (int k = 0; k < 18; k++)
#pragma unroll
      for (int n = 0; n < 3; n++) acc[n] = MFMA16(Af[k], *(const bfx8*)(sm + (n * 18 + k) * 1024 + lane * 16), acc[n]);
    float* res = (float*)(sm + 56 * 1024);
#pragma unroll
    for (int n = 0; n < 3; n++)
#pragma unroll
      for (int r = 0; r < 4; r++) res[(w * 16 + g * 4 + r) * 49 + n * 16 + i] = acc[n][r];
    __syncthreads();
    char* kv = ws + WS_KV + (size_t)rt * 4 * 3584;
    if (tid < 128) {
      float s = res[tid * 49 + 36];
      ((float*)(ws + WS_G))[rt * 128 + tid] = __builtin_amdgcn_rcpf(1.0f + __builtin_amdgcn_exp2f(-1.44269504f * s));
    }
    {
      const int tile = w >> 1, r = lane & 31, h = lane >> 5;
      const float* rb = res + tile * 32 * 49;
      char* dst = kv + tile * 3584;
      if ((w & 1) == 0) {
        const float* p = rb + r * 49 + 8 * h;
        u32x4 o = {pk2(p[0], p[1]), pk2(p[2], p[3]), pk2(p[4], p[5]), pk2(p[6], p[7])};
        *(u32x4*)(dst + lane * 16) = o;
        u32x2 o2;
        if (h == 0) o2 = u32x2{pk2(rb[r * 49 + 16], rb[r * 49 + 17]), 0x3f803f80u};
        else o2 = u32x2{0x00003f80u, 0u};
        *(u32x2*)(dst + 3072 + lane * 8) = o2;
      } else {
#pragma unroll
        for (int s = 0; s < 2; s++) {
          float v[8];
#pragma unroll
          for (int j = 0; j < 8; j++) {
            int key = 16 * s + 8 * (j >> 2) + 4 * h + (j & 3);
            float t = rb[key * 49 + 18 + (r < 18 ? r : 0)];
            v[j] = r < 18 ? t : ((r == 18 || r == 20) ? 1.0f : 0.0f);
          }
          u32x4 o = {pk2(v[0], v[1]), pk2(v[2], v[3]), pk2(v[4], v[5]), pk2(v[6], v[7])};
          *(u32x4*)(dst + 1024 + s * 1024 + lane * 16) = o;
        }
      }
    }
    return;
  }
  if (w >= 4) {
    const int lw = w - 4;
    const u16* W = ct < 12 ? w_in + FH + ct * 192 : wq + (ct - 12) * 192;
    const int ldw = ct < 12 ? 4608 : 576;
    const int r8 = lane >> 3, p = lane & 7;
    const u16* asrc[4];
    const u16* bsrc[6];
#pragma unroll
    for (int j = 0; j < 4; j++) {
      int row = (lw * 4 + j) * 8 + r8;
      int f = (row & 3) | (((row >> 3) & 1) << 2);
      asrc[j] = x + (size_t)(rt * 128 + row) * 576 + (p ^ f) * 8;
    }
#pragma unroll
    for (int j = 0; j < 6; j++) {
      int st = lw * 6 + j, ng = st >> 3, kb = st & 7;
      int h = ((r8 >> 1) & 1) | ((kb & 1) << 1);
      bsrc[j] = W + (size_t)(kb * 8 + r8) * ldw + ng * 64 + (p ^ (h << 1)) * 8;
    }
    auto issue = [&](int kt) {
      char* st = sm + (kt % K1_NS) * K1_ST;
#pragma unroll
      for (int j = 0; j < 4; j++) glds16(asrc[j] + kt * 64, st + (lw * 4 + j) * 1024);
#pragma unroll
      for (int j = 0; j < 6; j++) glds16(bsrc[j] + (size_t)kt * 64 * ldw, st + 16384 + (lw * 6 + j) * 1024);
    };
    issue(0); issue(1); issue(2);
    WAITV(20); BAR();
#pragma unroll
    for (int kt = 0; kt < 9; kt++) {
      if (kt < 7) WAITV(10); else WAITV(0);
      BAR();
      if (kt + 3 < 9) issue(kt + 3);
    }
    return;
  }
  if (ct < 12) k1_consumer<false>(sm, w, lane, rt, ct, ws);
  else k1_consumer<true>(sm, w, lane, rt, ct, ws);
}

#define ACQW 4
#define BPRIO 1
template <bool B> struct BoolC { static constexpr bool value = B; };
template <int CTRL>
__device__ __forceinline__ float dpp_add(float v) {
  return v + __builtin_bit_cast(float, __builtin_amdgcn_update_dpp(0, __builtin_bit_cast(int, v), CTRL, 0xf, 0xf, true));
}
#define POLL4(c) do { while (__hip_atomic_load(&(c), __ATOMIC_RELAXED, __HIP_MEMORY_SCOPE_WORKGROUP) < 4u) __builtin_amdgcn_s_sleep(1); asm volatile("" ::: "memory"); } while (0)
#define PIN(x) asm volatile("" : "+v"(x))
#define EX2(x) __builtin_amdgcn_exp2f(x)
#define ATT_STEP(sc, sn, K0, K1, Q0, Q1, Ocur, V0, Oprev, V1p, pprev, pcur)                          \
  {                                                                                                  \
    u32x4 pa;                                                                                        \
    sn = MFMA32(K0, Q0, zero16); PIN(sn); SCHED0();                                                  \
    { float e0 = EX2(sc[0]), e1 = EX2(sc[1]), e2 = EX2(sc[2]), e3 = EX2(sc[3]);                      \
      pa[0] = pk2(e0, e1); pa[1] = pk2(e2, e3); } PIN(pa); SCHED0();                                  \
    sn = MFMA32H(K1, __builtin_bit_cast(s16x4, Q1), sn); PIN(sn); SCHED0();                          \
    { float e0 = EX2(sc[4]), e1 = EX2(sc[5]), e2 = EX2(sc[6]), e3 = EX2(sc[7]);                      \
      pa[2] = pk2(e0, e1); pa[3] = pk2(e2, e3); } PIN(pa); SCHED0();                                  \
    Oprev = MFMA32(V1p, __builtin_bit_cast(bfx8, pprev), Oprev); PIN(Oprev); SCHED0();               \
    { float e0 = EX2(sc[8]), e1 = EX2(sc[9]), e2 = EX2(sc[10]), e3 = EX2(sc[11]);                    \
      pcur[0] = pk2(e0, e1); pcur[1] = pk2(e2, e3); } PIN(pcur); SCHED0();                           \
    Ocur = MFMA32(V0, __builtin_bit_cast(bfx8, pa), Ocur); PIN(Ocur); SCHED0();                      \
    { float e0 = EX2(sc[12]), e1 = EX2(sc[13]), e2 = EX2(sc[14]), e3 = EX2(sc[15]);                  \
      pcur[2] = pk2(e0, e1); pcur[3] = pk2(e2, e3); } PIN(pcur); SCHED0();                           \
  }

constexpr int K3_ST = 34816;

template <int NT>
__device__ __forceinline__ void k4_body(char* sm, char* ws, __amdgpu_buffer_rsrc_t rs_, u16* out, int rt, int nt0, int w, int lane STPARM) {
  f32x4 acc[4][NT];
#pragma unroll
  for (int m = 0; m < 4; m++)
#pragma unroll
    for (int n = 0; n < NT; n++) acc[m][n] = f32x4{0.f, 0.f, 0.f, 0.f};
  bfx8 Af[3][4], Bf[3][NT];
  const unsigned ap = (unsigned)WS_AH + rt * 4 * 90 * 1024 + lane * 16;
  const char* bp = ws + WS_WO + (size_t)nt0 * 90 * 1024 + lane * 16;
  auto lda = [&](int buf, int k) {
#pragma unroll
    for (int m = 0; m < 4; m++) Af[buf][m] = __builtin_bit_cast(bfx8, LD16(ap, (m * 90 + k) * 1024));
  };
  auto ldb = [&](int buf, int k) {
#pragma unroll
    for (int n = 0; n < NT; n++) Bf[buf][n] = *(const bfx8*)(bp + (n * 90 + k) * 1024);
  };
  auto mm = [&](int buf) {
#pragma unroll
    for (int m = 0; m < 4; m++)
#pragma unroll
      for (int n = 0; n < NT; n++) acc[m][n] = MFMA16(Bf[buf][n], Af[buf][m], acc[m][n]);
  };
  if (w >= 4) {
#pragma unroll
    for (int u = 0; u < 2; u++)
#pragma unroll
      for (int k = 0; k < 3; k++)
#pragma unroll
        for (int n = 0; n < NT; n++) {
          const int ww = (w - 4) * 2 + u;
          glds16(bp + (n * 90 + 18 + ww * 9 + 3 + k) * 1024, sm + (ww * 4 * NT + k * NT + n) * 1024);
        }
    const int k0 = w < 6 ? (w - 4) * 5 : 10 + (w - 6) * 4;
    ldb(0, k0); ldb(1, k0 + 1); ldb(2, k0 + 2);
    AWAIT(1 + rt, 8u);
    lda(0, k0); lda(1, k0 + 1); lda(2, k0 + 2);
    SCHED0(); mm(0); SCHED0(); lda(0, k0 + 3); ldb(0, k0 + 3);
    SCHED0(); mm(1); SCHED0();
    if (w < 6) { lda(1, k0 + 4); ldb(1, k0 + 4); }
    SCHED0(); mm(2); SCHED0(); mm(0); SCHED0();
    if (w < 6) mm(1);
    SCHED0();
  }
  const int k0 = 18 + w * 9;
  ldb(0, k0); ldb(1, k0 + 1); ldb(2, k0 + 2);
  __builtin_amdgcn_s_waitcnt(0x0F70);
  STAMP(1, 1);
  if (w == 4) AWAIT(33 + (rt >> 1), 16u);
  BAR(); asm volatile("" ::: "memory");
  STAMP(1, 2);
  lda(0, k0); lda(1, k0 + 1); lda(2, k0 + 2);
  bfx8 Bl[NT];
  auto rdl = [&](int k) {
#pragma unroll
    for (int n = 0; n < NT; n++) Bl[n] = *(const bfx8*)(sm + (w * 4 * NT + k * NT + n) * 1024 + lane * 16);
  };
  auto mml = [&](int buf) {
#pragma unroll
    for (int m = 0; m < 4; m++)
#pragma unroll
      for (int n = 0; n < NT; n++) acc[m][n] = MFMA16(Bl[n], Af[buf][m], acc[m][n]);
  };
  rdl(0);
  SCHED0(); mm(0); SCHED0(); lda(0, k0 + 3); ldb(0, k0 + 6);
  SCHED0(); mm(1); SCHED0(); lda(1, k0 + 4); ldb(1, k0 + 7);
  SCHED0(); mm(2); SCHED0(); lda(2, k0 + 5); ldb(2, k0 + 8);
  SCHED0(); mml(0); SCHED0(); lda(0, k0 + 6); rdl(1);
  SCHED0(); mml(1); SCHED0(); lda(1, k0 + 7); rdl(2);
  SCHED0(); mml(2); SCHED0(); lda(2, k0 + 8);
  SCHED0(); mm(0); SCHED0(); mm(1); SCHED0(); mm(2); SCHED0();
  STAMP(1, 3);
#pragma unroll
  for (int m = 0; m < 4; m++)
#pragma unroll
    for (int n = 0; n < NT; n++) *(f32x4*)(sm + (w * 4 * NT + m * NT + n) * 1024 + lane * 16) = acc[m][n];
  WAITL0(); BAR();
  for (int c = w * 64 + lane; c < 128 * NT; c += 512) {
    const int fr = c >> 5, i = c & 15, gp = (c >> 4) & 1;
    const char* p = sm + fr * 1024 + (i + 32 * gp) * 16;
    f32x4 s0 = *(const f32x4*)p, s1 = *(const f32x4*)(p + 256);
#pragma unroll
    for (int ww = 1; ww < 8; ww++) { s0 += *(const f32x4*)(p + ww * 4 * NT * 1024); s1 += *(const f32x4*)(p + ww * 4 * NT * 1024 + 256); }
    const int m = fr / NT, n = fr % NT;
    *(u32x4*)(out + (size_t)(rt * 64 + m * 16 + i) * 576 + (nt0 + n) * 16 + gp * 8) =
        u32x4{pk2(s0[0], s0[1]), pk2(s0[2], s0[3]), pk2(s1[0], s1[1]), pk2(s1[2], s1[3])};
  }
}

__global__ __launch_bounds__(512, 2) void k2_main(const u16* __restrict__ x, const u16* __restrict__ w_in,
                                                  const u16* __restrict__ wo, const u16* __restrict__ w_out,
                                                  const u16* __restrict__ w_sp, const u16* __restrict__ b_sp,
                                                  char* __restrict__ ws, u16* __restrict__ out) {
  __shared__ __attribute__((aligned(1024))) char sm[163840];
  char* kvs = sm + 20480;
  float* part = (float*)(sm + 72704);
  unsigned* obuf = (unsigned*)(sm + 154624);
  float* red = (float*)sm;
  unsigned* cnt = (unsigned*)(sm + 1024);
  const __amdgpu_buffer_rsrc_t rs_ = __builtin_amdgcn_make_buffer_rsrc(ws, 0, (unsigned)WS_END, 0x00020000);
  const int tid = threadIdx.x, lane = tid & 63;
  const int w = __builtin_amdgcn_readfirstlane(tid >> 6);
  const int b = blockIdx.x;
  const int r = lane & 31, h = lane >> 5;
  const int qh = w & 1, ksp = w >> 1;
  STAMP0(); STAMP(1, 0);
  const unsigned l16 = lane * 16, l8 = lane * 8;
  const int ntile = w < 4 ? 20 : 12;
  const char* kvu = ws + WS_KV + (size_t)(w < 4 ? ksp * 20 : 16 + ksp * 12) * 3584;
#define KV0(t, o) (*(const bfx8*)(kvu + ((t) * 3584 + (o)) + l16))
#define KV1(t) (*(const s16x4*)(kvu + ((t) * 3584 + 3072) + l8))
  bfx8 q0[4]; u32x2 q1[4]; unsigned qd[4];
  bfx8 kA; s16x4 kA1;
  auto ldq = [&]() {
#pragma unroll
  for (int a = 0; a < 4; a++) {
    const char* qp = ws + WS_Q + ((size_t)(b * 8 + qh * 4 + a) * 576 + r * 18) * 2;
    q0[a] = __builtin_bit_cast(bfx8, (u32x4) * (const u32x4_a4*)(qp + 16 * h));
    qd[a] = *(const unsigned*)(qp + 32);
  }
  kA = KV0(0, 0); kA1 = KV1(0);
  };
  const int xcd = b & 7, ii = b >> 3;
  const int rt = (xcd & 1) * 8 + (ii & 7), ct = (xcd >> 1) * 4 + (ii >> 3);
  float og[2], bs[2], gt2[2];
  if (w < 4) {
    ldq();
#pragma unroll
    for (int m = 0; m < 2; m++) {
      const int t = rt * 128 + w * 32 + m * 16 + (lane & 15);
      og[m] = ((const float*)(ws + WS_G))[t];
      bs[m] = __uint_as_float((unsigned)b_sp[t]);
      gt2[m] = ((const float*)(ws + WS_G))[b * 8 + w + 4 * m];
    }
    SCHED0();
  }
  if (tid < 4) cnt[tid] = 0u;
  WAITL0(); BAR();
  if (w >= 4) {
    const int pc0 = (w & 1) * 70 + ((w >> 1) & 1);
#pragma unroll
    for (int j = 0; j < 35; j++) glds16(ws + WS_KV + (pc0 + 2 * j) * 1024 + l16, kvs + (pc0 + 2 * j) * 1024);
  }
  if (w >= 4) {
    const int hw = w - 4, i = lane & 15, g = lane >> 4;
    __builtin_amdgcn_s_setprio(3);
    WAITV(28);
    if (lane == 0) asm volatile("ds_add_u32 %0, %1" ::"v"((unsigned)(uintptr_t)(LDSP unsigned*)&cnt[1]), "v"(1u) : "memory");
    WAITV(0);
    if (lane == 0) __hip_atomic_fetch_add(&cnt[2], 1u, __ATOMIC_RELAXED, __HIP_MEMORY_SCOPE_WORKGROUP);
    char* zu = ws + WS_Z2 + (size_t)b * 256 + (size_t)hw * 36 * 65536;
    const unsigned zo = i * 16 + g * 65536;
    u32x4 zr[9];
#pragma unroll
    for (int c = 0; c < 9; c++) zr[c] = *(const u32x4*)(zu + c * 4 * 65536 + zo);
    u32x2 wv_[2][8];
    unsigned wdst[2], wstr[2];
    const int job0 = hw * 256 + b;
#pragma unroll
    for (int u = 0; u < 2; u++) {
      const int job = u == 0 ? job0 : (job0 < 434 ? job0 + 1024 : job0);
      const u16* wsrc; int wld, whi = 4;
      if (job < 648) {
        int kt = job / 36, cb = job % 36;
        wsrc = w_in + (size_t)(kt * 32 + g * 8) * 4608 + cb * 64 + i * 4; wld = 4608;
        wdst[u] = (unsigned)WS_W1 + (cb * 4 * 18 + kt) * 1024; wstr[u] = 18 * 1024;
      } else {
        int j2 = job - 648, kt = j2 / 9, cb = j2 % 9;
        int c2 = kt - 18, pp = c2 / 9, c9 = c2 % 9;
        int f0 = pp * 288 + (c9 < 5 ? c9 * 32 : 144 + (c9 - 5) * 32) + 4 * g;
        if (kt >= 18) whi = c9 == 4 ? 144 : 16;
        wsrc = (kt < 18 ? wo + (size_t)(kt * 32 + g * 8) * 576 : w_out + (size_t)f0 * 576) + cb * 64 + i * 4;
        wld = 576;
        wdst[u] = (unsigned)WS_WO + (cb * 4 * 90 + kt) * 1024; wstr[u] = 90 * 1024;
      }
#pragma unroll
      for (int j = 0; j < 8; j++) wv_[u][j] = *(const u32x2*)(wsrc + (size_t)(j < 4 ? j : j - 4 + whi) * wld);
    }
    const char* pf = (const char*)w_sp + (size_t)b * 32768 + (tid - 256) * 64;
    const unsigned pf0 = *(const unsigned*)pf, pf1 = *(const unsigned*)(pf + 16384);
    SCHED0();
    float s1[8], s2[8];
#pragma unroll
    for (int j = 0; j < 8; j++) s1[j] = s2[j] = 0.f;
#pragma unroll
    for (int c = 0; c < 9; c++)
#pragma unroll
      for (int j = 0; j < 4; j++) {
        float a = bflo(zr[c][j]), bb = bfhi(zr[c][j]);
        s1[2 * j] += a; s2[2 * j] = __builtin_fmaf(a, a, s2[2 * j]);
        s1[2 * j + 1] += bb; s2[2 * j + 1] = __builtin_fmaf(bb, bb, s2[2 * j + 1]);
      }
#pragma unroll
    for (int j = 0; j < 8; j++) {
      s1[j] = dpp_add<0x121>(dpp_add<0x122>(dpp_add<0x124>(dpp_add<0x128>(s1[j]))));
      s2[j] = dpp_add<0x121>(dpp_add<0x122>(dpp_add<0x124>(dpp_add<0x128>(s2[j]))));
    }
    if (i == 0) {
      float* p = red + (hw * 4 + g) * 16;
      *(f32x4*)p = f32x4{s1[0], s1[1], s1[2], s1[3]}; *(f32x4*)(p + 4) = f32x4{s1[4], s1[5], s1[6], s1[7]};
      *(f32x4*)(p + 8) = f32x4{s2[0], s2[1], s2[2], s2[3]}; *(f32x4*)(p + 12) = f32x4{s2[4], s2[5], s2[6], s2[7]};
    }
    WAITL0();
    if (lane == 0) __hip_atomic_fetch_add(&cnt[0], 1u, __ATOMIC_RELAXED, __HIP_MEMORY_SCOPE_WORKGROUP);
#pragma unroll
    for (int u = 0; u < 2; u++) {
#pragma unroll
      for (int j = 0; j < 8; j++) PIN(wv_[u][j]);
      if (u == 0 || job0 < 434) {
#pragma unroll
        for (int c = 0; c < 2; c++) {
          u32x4 lo, hi;
#pragma unroll
          for (int j = 0; j < 4; j++) {
            lo[j] = __builtin_amdgcn_perm(wv_[u][2 * j + 1][c], wv_[u][2 * j][c], 0x05040100u);
            hi[j] = __builtin_amdgcn_perm(wv_[u][2 * j + 1][c], wv_[u][2 * j][c], 0x07060302u);
          }
          char* bq = sm + 2048 + hw * 4096 + (i >> 2) * 1024 + ((i & 3) * 4 + g * 16) * 16 + c * 32;
          *(u32x4*)bq = lo; *(u32x4*)(bq + 16) = hi;
        }
        u32x4 fv[4];
#pragma unroll
        for (int c = 0; c < 4; c++) fv[c] = *(const u32x4*)(sm + 2048 + hw * 4096 + c * 1024 + l16);
#pragma unroll
        for (int c = 0; c < 4; c++) ST16(fv[c], wdst[u] + c * wstr[u] + l16);
      }
    }
    asm volatile("" ::"v"(pf0), "v"(pf1));
    POLL4(cnt[0]);
    float t = 0.f;
#pragma unroll
    for (int j = 0; j < 4; j++) t += red[j * 64 + lane];
    t += __shfl_xor(t, 16, 64); t += __shfl_xor(t, 32, 64);
    float t2 = __builtin_bit_cast(float, __builtin_amdgcn_update_dpp(0, __builtin_bit_cast(int, t), 0x128, 0xf, 0xf, true));
    float mean = t * (1.0f / 2304.0f);
    float var = fmaxf(t2 * (1.0f / 2304.0f) - mean * mean, 0.f);
    float rsv = __builtin_amdgcn_rsqf(var + 1e-5f), muv = -mean * rsv;
    float rs[8], mu[8];
#pragma unroll
    for (int j = 0; j < 8; j++) {
      rs[j] = __builtin_bit_cast(float, __builtin_amdgcn_readlane(__builtin_bit_cast(int, rsv), j));
      mu[j] = __builtin_bit_cast(float, __builtin_amdgcn_readlane(__builtin_bit_cast(int, muv), j));
    }
#pragma unroll
    for (int c = 0; c < 9; c++) {
      u32x4 o;
#pragma unroll
      for (int j = 0; j < 4; j++)
        o[j] = pk2(__builtin_fmaf(bflo(zr[c][j]), rs[2 * j], mu[2 * j]), __builtin_fmaf(bfhi(zr[c][j]), rs[2 * j + 1], mu[2 * j + 1]));
      ST16(o, (unsigned)WS_ZN + b * 256 + (hw * 36 + c * 4) * 65536 + zo);
    }
    SCHED0();
    ldq();
  } else {
    PIN(og[0]); PIN(og[1]); PIN(bs[0]); PIN(bs[1]);
  }
  STAMP(1, 1);
  bfx8 vA0, vA1;
  f32x16 O[4], sA, sB;
  float mrow[4];
  u32x4 pX, pY;
  bfx8 kB, vB0, vB1;
  s16x4 kB1;
  const f32x16 zero16 = {0.f, 0.f, 0.f, 0.f, 0.f, 0.f, 0.f, 0.f, 0.f, 0.f, 0.f, 0.f, 0.f, 0.f, 0.f, 0.f};
#pragma unroll
  for (int a = 0; a < 4; a++) qd[a] = h == 0 ? qd[a] : 0u;
#pragma unroll
  for (int a = 0; a < 4; a++) {
    f32x16 s = MFMA32(kA, q0[a], zero16);
    s = MFMA32H(kA1, __builtin_bit_cast(s16x4, u32x2{qd[a], 0u}), s);
    float m = s[0];
#pragma unroll
    for (int j = 1; j < 16; j++) m = fmaxf(m, s[j]);
    mrow[a] = m + 64.0f;
    PIN(mrow[a]);
  }
  auto setup = [&]() {
#pragma unroll
    for (int a = 0; a < 4; a++) {
      auto sw = __builtin_amdgcn_permlane32_swap(__float_as_uint(mrow[a]), __float_as_uint(mrow[a]), false, false);
      float m = fmaxf(__uint_as_float(sw[0]), __uint_as_float(sw[1]));
      mrow[a] = m;
      float nm = -m;
      unsigned hi = __float_as_uint(nm) & 0xffff0000u;
      float r1 = nm - __uint_as_float(hi);
      unsigned mid = __float_as_uint(r1) & 0xffff0000u;
      float r2 = r1 - __uint_as_float(mid);
      unsigned lo = __float_as_uint(r2) & 0xffff0000u;
      q1[a] = h == 0 ? u32x2{qd[a], (hi >> 16) | mid} : u32x2{lo >> 16, 0u};
      O[a] = zero16;
    }
    sA = MFMA32(kA, q0[0], zero16);
    sA = MFMA32H(kA1, __builtin_bit_cast(s16x4, q1[0]), sA);
    pX = u32x4{0u, 0u, 0u, 0u};
    vB1 = vA0;
    SCHED0();
  };
  const char* kvl = kvs + ksp * 20 * 3584;
#define KL0(t, o) (LD ? *(const bfx8*)(kvl + ((t) * 3584 + (o)) + l16) : KV0(t, o))
#define KL1(t) (LD ? *(const s16x4*)(kvl + ((t) * 3584 + 3072) + l8) : KV1(t))
  auto pair = [&](int t, auto ldc) {
    constexpr bool LD = decltype(ldc)::value;
    kB = KL0(t + 1, 0); kB1 = KL1(t + 1); vB0 = KL0(t + 1, 1024);
    SCHED0();
    ATT_STEP(sA, sB, kA, kA1, q0[1], q1[1], O[0], vA0, O[3], vB1, pX, pY);
    vB1 = KL0(t + 1, 2048);
    SCHED0();
    ATT_STEP(sB, sA, kA, kA1, q0[2], q1[2], O[1], vA0, O[0], vA1, pY, pX);
    ATT_STEP(sA, sB, kA, kA1, q0[3], q1[3], O[2], vA0, O[1], vA1, pX, pY);
    ATT_STEP(sB, sA, kB, kB1, q0[0], q1[0], O[3], vA0, O[2], vA1, pY, pX);
    const int t2 = t + 2 < ntile ? t + 2 : t + 1;
    kA = KL0(t2, 0); kA1 = KL1(t2); vA0 = KL0(t2, 1024);
    SCHED0();
    ATT_STEP(sA, sB, kB, kB1, q0[1], q1[1], O[0], vB0, O[3], vA1, pX, pY);
    vA1 = KL0(t2, 2048);
    SCHED0();
    ATT_STEP(sB, sA, kB, kB1, q0[2], q1[2], O[1], vB0, O[0], vB1, pY, pX);
    ATT_STEP(sA, sB, kB, kB1, q0[3], q1[3], O[2], vB0, O[1], vB1, pX, pY);
    ATT_STEP(sB, sA, kA, kA1, q0[0], q1[0], O[3], vB0, O[2], vB1, pY, pX);
  };
  for (int attempt = 0; attempt < 2; attempt++) {
    if (w < 4) { POLL4(cnt[1]); vA0 = *(const bfx8*)(kvl + 1024 + l16); vA1 = *(const bfx8*)(kvl + 2048 + l16); SCHED0(); }
    else {
      vA0 = KV0(0, 1024); vA1 = KV0(0, 2048);
      if (attempt == 0) {
        WAITV(0);
        if (lane == 0 && __hip_atomic_fetch_add(&cnt[3], 1u, __ATOMIC_RELAXED, __HIP_MEMORY_SCOPE_WORKGROUP) == 3u) SIGNAL(0);
      }
    }
    setup();
    STAMP(1, 2);
    if (w < 4) { for (int t = 0; t < 20; t += 2) { if (t == 2) POLL4(cnt[2]); pair(t, BoolC<true>{}); } }
    else {
      __builtin_amdgcn_s_setprio(BPRIO);
      for (int t = 0; t < 12; t += 2) pair(t, BoolC<false>{});
    }
    O[3] = MFMA32(vB1, __builtin_bit_cast(bfx8, pX), O[3]);
    float chk = 0.f, lmax = 0.f;
#pragma unroll
    for (int a = 0; a < 4; a++) {
#pragma unroll
      for (int j = 0; j < 11; j++) chk += fabsf(O[a][j]);
      lmax = fmaxf(lmax, h == 0 ? O[a][10] : O[a][8]);
    }
    bool bad = !(lmax < 1.0e33f) || !(chk < 3.0e38f);
    if (attempt == 1 || !__any(bad)) break;
#pragma unroll
    for (int a = 0; a < 4; a++) mrow[a] = -INFINITY;
    for (int t = 0; t < ntile; t++) {
      bfx8 k0 = KV0(t, 0);
      s16x4 k1 = KV1(t);
#pragma unroll
      for (int a = 0; a < 4; a++) {
        f32x16 s = MFMA32(k0, q0[a], zero16);
        s = MFMA32H(k1, __builtin_bit_cast(s16x4, u32x2{qd[a], 0u}), s);
#pragma unroll
        for (int j = 0; j < 16; j++) mrow[a] = fmaxf(mrow[a], s[j]);
      }
    }
    kA = KV0(0, 0); kA1 = KV1(0);
  }
  STAMP(1, 3);
  __builtin_amdgcn_s_setprio(0);
  if (w == ACQW) { AWAIT(0, 256u); __builtin_amdgcn_fence(__ATOMIC_ACQUIRE, "agent"); WAITV(0); }
  WAITL0(); BAR();
#pragma unroll
  for (int a = 0; a < 4; a++) {
    float* p = part + ((ksp * 8 + qh * 4 + a) * 32 + r) * 20;
    *(f32x4*)(p + 4 * h) = f32x4{O[a][0], O[a][1], O[a][2], O[a][3]};
    *(f32x4*)(p + 8 + 4 * h) = f32x4{O[a][4], O[a][5], O[a][6], O[a][7]};
    if (h == 0) *(f32x4*)(p + 16) = f32x4{O[a][8], O[a][9], O[a][10], mrow[a]};
  }
  WAITL0(); BAR();
  if (w >= 4) {
    const int lw = w - 4, r8 = lane >> 3, p = lane & 7;
    const int nb = lw < 2 ? 5 : 4, b0 = lw < 2 ? lw * 5 : 10 + (lw - 2) * 4;
    const char* a1 = (const char*)x + (size_t)(rt * 128 + lw * 32 + r8) * 1152 + (p ^ r8) * 16;
    const char* a2 = (const char*)w_sp + (size_t)(rt * 128 + lw * 32 + r8) * 4096 + (p ^ r8) * 16;
    const char* b1 = ws + WS_W1 + (size_t)ct * 9 * 18 * 1024 + lane * 16;
    const char* b2 = ws + WS_ZN + (size_t)ct * 9 * 64 * 1024 + lane * 16;
    auto issue = [&](int j) {
      char* st = sm + (j & 3) * K3_ST;
      if (j < 9) {
#pragma unroll
        for (int u = 0; u < 4; u++) glds16(a1 + u * 8 * 1152 + j * 128, st + (lw * 4 + u) * 1024);
#pragma unroll
        for (int u = 0; u < 5; u++)
          if (u < nb) { int pc = b0 + u; glds16(b1 + ((pc >> 1) * 18 + 2 * j + (pc & 1)) * 1024, st + 16384 + pc * 1024); }
      } else {
        const int kk = j - 9;
#pragma unroll
        for (int u = 0; u < 4; u++) glds16(a2 + u * 8 * 4096 + kk * 128, st + (lw * 4 + u) * 1024);
#pragma unroll
        for (int u = 0; u < 5; u++)
          if (u < nb) { int pc = b0 + u; glds16(b2 + ((pc >> 1) * 64 + 2 * kk + (pc & 1)) * 1024, st + 16384 + pc * 1024); }
      }
    };
    issue(0); issue(1);
    BAR();
    if (lw < 2) WAITV(9); else WAITV(8);
    BAR();
    issue(2); issue(3);
    for (int j = 0; j < 38; j++) {
      if (lw < 2) WAITV(18); else WAITV(16);
      BAR();
      if (j + 4 < 41) issue(j + 4);
    }
    if (lw < 2) WAITV(9); else WAITV(8);
    BAR();
    WAITV(0); BAR();
    BAR();
    STAMP(1, 4); STAMP(1, 5); STAMP(1, 6);
  } else {
    const int i = lane & 15, g = lane >> 4;
    og[0] = 1.0f - og[0]; og[1] = 1.0f - og[1];
    bs[0] = bflo(__float_as_uint(bs[0])); bs[1] = bflo(__float_as_uint(bs[1]));
#pragma unroll
    for (int u = 0; u < 2; u++) {
      const int tok = w + 4 * u;
      const float gt = gt2[u];
      const float* p = part + (tok * 32 + r) * 20;
      f32x4 tl[4]; float L[4], M = -INFINITY;
#pragma unroll
      for (int k = 0; k < 4; k++) {
        tl[k] = *(const f32x4*)(p + k * 5120 + 16);
        L[k] = tl[k][3] + __builtin_amdgcn_logf(tl[k][2]);
        M = fmaxf(M, L[k]);
      }
      float den = 0.f;
#pragma unroll
      for (int k = 0; k < 4; k++) { L[k] = EX2(L[k] - M); den += L[k]; }
      const float inv = gt * __builtin_amdgcn_rcpf(den);
      f32x4 o0 = {0.f, 0.f, 0.f, 0.f}, o1 = o0; float o2 = 0.f, o3 = 0.f;
#pragma unroll
      for (int k = 0; k < 4; k++) {
        float sc = L[k] * inv * __builtin_amdgcn_rcpf(tl[k][2]);
        o0 += *(const f32x4*)(p + k * 5120 + 8 * h) * sc;
        o1 += *(const f32x4*)(p + k * 5120 + 8 * h + 4) * sc;
        o2 += tl[k][0] * sc; o3 += tl[k][1] * sc;
      }
      unsigned* ob = obuf + tok * 288 + r * 9 + 4 * h;
      ob[0] = pk2(o0[0], o0[1]); ob[1] = pk2(o0[2], o0[3]); ob[2] = pk2(o1[0], o1[1]); ob[3] = pk2(o1[2], o1[3]);
      if (h == 0) ob[8] = pk2(o2, o3);
    }
    WAITL0(); BAR();
    unsigned* cnt2 = (unsigned*)(sm + 160000);
    {
      u32x4 ov[3];
#pragma unroll
      for (int u = 0; u < 3; u++) { int c = tid + u * 256; c = c < 576 ? c : 575; ov[u] = *(const u32x4*)(obuf + (c & 7) * 288 + (c >> 3) * 4); }
#pragma unroll
      for (int u = 0; u < 3; u++) {
        const int c = tid + u * 256, ch = c >> 3, t = b * 8 + (c & 7);
        if (u < 2 || w == 0) ST16(ov[u], (unsigned)WS_AH + ((t >> 4) * 90 + (ch >> 2)) * 1024 + ((t & 15) + 16 * (ch & 3)) * 16);
      }
    }
    f32x4 acc[9][2], z1[9][2];
    unsigned z1p[9][2][2];
#pragma unroll
    for (int n = 0; n < 9; n++) acc[n][0] = acc[n][1] = f32x4{0.f, 0.f, 0.f, 0.f};
    const int aoff0 = (w * 32 + i) * 128 + ((g ^ (i & 7)) * 16), aoff1 = aoff0 ^ 64;
    const int boff = 16384 + lane * 16;
    bfx8 Af[2][2], Bf[2][9];
    auto rd = [&](int buf, char* st, int ks) {
#pragma unroll
      for (int m = 0; m < 2; m++) Af[buf][m] = *(const bfx8*)(st + (ks ? aoff1 : aoff0) + m * 2048);
#pragma unroll
      for (int n = 0; n < 9; n++) Bf[buf][n] = *(const bfx8*)(st + boff + (n * 2 + ks) * 1024);
    };
    auto mm = [&](int buf) {
#pragma unroll
      for (int n = 0; n < 9; n++)
#pragma unroll
        for (int m = 0; m < 2; m++) acc[n][m] = MFMA16(Bf[buf][n], Af[buf][m], acc[n][m]);
    };
    auto ilv = [&](bool gl) {
#pragma unroll
      for (int x2 = 0; x2 < 18; x2++) {
        __builtin_amdgcn_sched_group_barrier(0x8, 1, 0);
        if (x2 < 11) __builtin_amdgcn_sched_group_barrier(0x100, 1, 0);
        if (gl) __builtin_amdgcn_sched_group_barrier(0x402, 1, 0);
      }
    };
    auto body = [&](int j, int gi) {
      int so0 = (j & 3) * K3_ST, so1 = ((j + 1) & 3) * K3_ST;
      asm volatile("" : "+s"(so0), "+s"(so1));
      if (gi >= 0) PIN(z1[gi >> 1][gi & 1]);
      rd(1, sm + so0, 1); mm(0);
      if (gi >= 0) { f32x4 v = z1[gi >> 1][gi & 1]; z1p[gi >> 1][gi & 1][0] = pk2(gelu_t(v[0]) * og[gi & 1], gelu_t(v[1]) * og[gi & 1]); }
      ilv(gi >= 0);
      SCHED0();
      if (gi >= 0) { PIN(z1p[gi >> 1][gi & 1][0]); PIN(z1[gi >> 1][gi & 1]); }
      WAITL0(); BAR(); SCHED0();
      rd(0, sm + so1, 0);
      mm(1);
      if (gi >= 0) { f32x4 v = z1[gi >> 1][gi & 1]; z1p[gi >> 1][gi & 1][1] = pk2(gelu_t(v[2]) * og[gi & 1], gelu_t(v[3]) * og[gi & 1]); }
      ilv(gi >= 0);
      SCHED0();
      if (gi >= 0) PIN(z1p[gi >> 1][gi & 1][1]);
    };
    WAITL0(); BAR(); SCHED0();
    STAMP(1, 4);
    rd(0, sm, 0);
    SCHED0();
    for (int j = 0; j < 8; j++) body(j, -1);
    WAITV(0);
    body(8, -1);
    if (tid == 0) { SIGNAL(1 + (b >> 3)); *cnt2 = 0u; }
#pragma unroll
    for (int n = 0; n < 9; n++)
#pragma unroll
      for (int m = 0; m < 2; m++) { z1[n][m] = acc[n][m]; acc[n][m] = f32x4{0.f, 0.f, 0.f, 0.f}; }
    SCHED0();
#pragma unroll
    for (int j = 9; j < 27; j++) body(j, j - 9);
    for (int j = 27; j < 41; j++) body(j, -1);
    STAMP(1, 5);
#pragma unroll
    for (int m = 0; m < 2; m++) {
      const unsigned dst = (unsigned)WS_AH + ((rt * 8 + w * 2 + m) * 90 + 18 + (ct >> 1) * 9) * 1024 + lane * 16;
      unsigned hp[9][2];
#pragma unroll
      for (int n = 0; n < 9; n++) {
        f32x4 a = acc[n][m];
        hp[n][0] = pk2(bflo(z1p[n][m][0]) * (a[0] + bs[m]), bfhi(z1p[n][m][0]) * (a[1] + bs[m]));
        hp[n][1] = pk2(bflo(z1p[n][m][1]) * (a[2] + bs[m]), bfhi(z1p[n][m][1]) * (a[3] + bs[m]));
        if (n & 1) ST16((u32x4{hp[n - 1][0], hp[n - 1][1], hp[n][0], hp[n][1]}), dst + ((ct & 1) * 5 + (n >> 1)) * 1024);
      }
      ST8((u32x2{hp[8][0], hp[8][1]}), dst + 4 * 1024 + (ct & 1) * 8);
    }
    WAITV(0);
    if (lane == 0 && __hip_atomic_fetch_add(cnt2, 1u, __ATOMIC_RELAXED, __HIP_MEMORY_SCOPE_WORKGROUP) == 3u) SIGNAL(33 + rt);
    STAMP(1, 6);
  }
  {
    const int rt4 = xcd * 4 + (ii & 3), cg = ii >> 2;
    const int nt0 = (cg >> 1) * 9 + (cg & 1) * 5;
    if (cg & 1) k4_body<4>(sm, ws, rs_, out, rt4, nt0, w, lane STARG);
    else k4_body<5>(sm, ws, rs_, out, rt4, nt0, w, lane STARG);
  }
  STAMPE(1, 7);
}

extern "C" void kernel_launch(void* const* d_in, const int* in_sizes, int n_in, void* d_out, int out_size,
                              void* d_ws, size_t ws_size, hipStream_t stream) {
  const u16* x = (const u16*)d_in[0];
  const u16* wq = (const u16*)d_in[1];
  const u16* wk = (const u16*)d_in[2];
  const u16* wv = (const u16*)d_in[3];
  const u16* wo = (const u16*)d_in[4];
  const u16* w_in = (const u16*)d_in[6];
  const u16* w_sp = (const u16*)d_in[7];
  const u16* b_sp = (const u16*)d_in[8];
  const u16* w_out = (const u16*)d_in[9];
  const u16* gw = (const u16*)d_in[10];
  char* ws = (char*)d_ws;
  k1_proj<<<256, 512, 0, stream>>>(x, wq, wk, wv, w_in, gw, ws);
  k2_main<<<256, 512, 0, stream>>>(x, w_in, wo, w_out, w_sp, b_sp, ws, (u16*)d_out);
}
```
